```python
import torch

def _softmax(x, axis=-1):
    return torch.softmax(x, dim=axis)

def _gelu(x):
    return torch.nn.functional.gelu(x, approximate='tanh')

def _silu(x):
    return torch.nn.functional.silu(x)

def _sigmoid(x):
    return torch.sigmoid(x)

def _layernorm(x, axis=-1, eps=1e-05):
    m = x.mean(dim=axis, keepdim=True)
    v = x.var(dim=axis, keepdim=True, unbiased=False)
    return (x - m) / torch.sqrt(v + eps)

def _scatter_add(tgt, idx, src, axis=0):
    return tgt.clone().index_add_(axis, idx, src)

def _segment_sum(src, seg, n):
    out = torch.zeros((n,) + src.shape[1:], dtype=src.dtype)
    return out.index_add_(0, seg, src)

def _pad(x, pw):
    flat = tuple((v for pair in reversed(pw) for v in pair))
    return torch.nn.functional.pad(x, flat)
kernel_name = 'parallel_sum__memory_aug_recurrent_mem_tr__glu_gated_reglu__prompt'

def setup_inputs():

    _w = lambda a: (lambda t: t.to(torch.bfloat16) if t.is_floating_point() else t)(torch.as_tensor(a))

    def _A_setup():
        B, S, Dm = (1, 2048, 640)
        H, Dh = (32, 192)
        M, seg = (8, 32)
        seg = min(seg, S)
        S = S // seg * seg
        return {'x': _w((lambda *s: torch.randn(*s, dtype=torch.bfloat16) * 0.05)(B, S, Dm)), 'mem0': _w((lambda *s: torch.randn(*s, dtype=torch.bfloat16) * 0.2)(B, M, Dm)), 'Wq': _w((lambda *s: torch.randn(*s, dtype=torch.bfloat16) * 1.6)(Dm, H, Dh) * Dm ** (-0.5)), 'Wk': _w((lambda *s: torch.randn(*s, dtype=torch.bfloat16) * 1.6)(Dm, H, Dh) * Dm ** (-0.5)), 'Wv': _w((lambda *s: torch.randn(*s, dtype=torch.bfloat16) * 0.2)(Dm, H, Dh) * Dm ** (-0.5)), 'Wo': _w((lambda *s: torch.randn(*s, dtype=torch.bfloat16) * 0.2)(H, Dh, Dm) * (H * Dh) ** (-0.5)), 'seg_len': seg}
    _A_in = _A_setup()

    def _B_setup():
        B, S, D, F = (1, 2048, 640, 1536)
        return {'x': _w((lambda *s: torch.randn(*s, dtype=torch.bfloat16) * 0.05)(B, S, D)), 'W_gate': _w((lambda *s: torch.randn(*s, dtype=torch.bfloat16) * 0.2)(D, F) * (2.0 / D) ** 0.5), 'W_up': _w((lambda *s: torch.randn(*s, dtype=torch.bfloat16) * 0.2)(D, F) * (2.0 / D) ** 0.5), 'W_down': _w((lambda *s: torch.randn(*s, dtype=torch.bfloat16) * 0.2)(F, D) * (1.0 / F) ** 0.5)}
    _B_in = _B_setup()
    return {'A_x': _A_in['x'], 'A_mem0': _A_in['mem0'], 'A_Wq': _A_in['Wq'], 'A_Wk': _A_in['Wk'], 'A_Wv': _A_in['Wv'], 'A_Wo': _A_in['Wo'], 'A_seg_len': _A_in['seg_len'], 'B_x': _B_in['x'], 'B_W_gate': _B_in['W_gate'], 'B_W_up': _B_in['W_up'], 'B_W_down': _B_in['W_down']}

def reference(A_x, A_mem0, A_Wq, A_Wk, A_Wv, A_Wo, A_seg_len, B_x, B_W_gate, B_W_up, B_W_down):

    def _A_ref(x, mem0, Wq, Wk, Wv, Wo, seg_len):
        B, S, Dm = x.shape
        H, Dh = (Wq.shape[1], Wq.shape[2])
        M = mem0.shape[1]
        scale = Dh ** (-0.5)
        n_seg = S // seg_len
        L = M + seg_len
        idx = torch.arange(L)
        mask = (idx[:, None] < M) | (idx[:, None] >= idx[None, :])
        mem = mem0
        outs = []
        for s in range(n_seg):
            xs = x[:, s * seg_len:(s + 1) * seg_len, :].clone()
            h = torch.concatenate([mem, xs], axis=1)
            q = torch.einsum('btd,dhe->bhte', h, Wq)
            k = torch.einsum('btd,dhe->bhte', h, Wk)
            v = torch.einsum('btd,dhe->bhte', h, Wv)
            scores = torch.einsum('bhqe,bhke->bhqk', q, k) * scale
            scores = torch.where(mask[None, None], scores, -1000000000.0)
            p = _softmax(scores, axis=-1)
            o = torch.einsum('bhqk,bhke->bhqe', p, v)
            o = torch.einsum('bhte,hed->btd', o, Wo)
            mem = o[:, :M, :].clone()
            outs.append(o[:, M:, :])
        return torch.concatenate(outs, axis=1)

    def _B_ref(x, W_gate, W_up, W_down):
        gate = torch.einsum('bsd,df->bsf', x, W_gate)
        up = torch.einsum('bsd,df->bsf', x, W_up)
        gate = gate * (lambda x, dt: x.to(dt))(gate > 0, gate.dtype)
        h = gate * up
        return torch.einsum('bsf,fd->bsd', h, W_down)
    x = A_x
    xn = _layernorm(x, axis=-1)
    return x + _A_ref(xn, A_mem0, A_Wq, A_Wk, A_Wv, A_Wo, A_seg_len) + _B_ref(xn, B_W_gate, B_W_up, B_W_down)
```

```cpp
#include <hip/hip_runtime.h>
#include <stdint.h>

typedef __bf16 v8bf __attribute__((ext_vector_type(8)));
typedef __bf16 v16bf __attribute__((ext_vector_type(16)));
typedef __bf16 v2bf __attribute__((ext_vector_type(2)));
typedef float v4f __attribute__((ext_vector_type(4)));
typedef float v2f __attribute__((ext_vector_type(2)));
typedef unsigned v4u __attribute__((ext_vector_type(4)));
typedef unsigned v2u __attribute__((ext_vector_type(2)));
typedef unsigned v8u __attribute__((ext_vector_type(8)));
typedef short v4s __attribute__((ext_vector_type(4)));
#define DEV __device__ __forceinline__
#define LDSP(T) __attribute__((address_space(3))) T*
typedef __amdgpu_buffer_rsrc_t rsrc_t;

constexpr size_t OFF_HAND = 0;
constexpr size_t OFF_PX = OFF_HAND + 64 * 65536;
constexpr size_t OFF_FFN = OFF_PX + 64 * 32 * 2560;
constexpr size_t POISON_END = OFF_FFN + 2048 * 640 * 2;
constexpr size_t OFF_FLAG = POISON_END;
constexpr size_t OFF_FLAGH = OFF_FLAG + 64 * 192 * 4;
constexpr size_t FLAG_END = OFF_FLAGH + 64 * 64 * 4;
constexpr size_t OFF_XNF = FLAG_END;
constexpr size_t OFF_UP = OFF_XNF + 2048 * 640 * 2;
constexpr size_t OFF_QKX = OFF_UP + (size_t)80 * 16 * 20 * 1024;
constexpr size_t OFF_HF = OFF_QKX + (size_t)64 * 32 * 24576;
constexpr size_t WS_END = OFF_HF + (size_t)128 * 48 * 1024;
constexpr unsigned SPIN_LIMIT = 400000u;
constexpr int LDS_BYTES = 163840;

template <int I> struct IC { static constexpr int v = I; };
template <int B, int E, class F> DEV void sfor(F&& f) { if constexpr (B < E) { f(IC<B>{}); sfor<B + 1, E>(f); } }

DEV unsigned pk(float a, float b) { v2f f = {a, b}; v2bf h = __builtin_convertvector(f, v2bf); return __builtin_bit_cast(unsigned, h); }
DEV float rbf(float a) { return (float)(__bf16)a; }
DEV float blo(unsigned u) { return __uint_as_float(u << 16); }
DEV float bhi(unsigned u) { return __uint_as_float(u & 0xffff0000u); }
DEV unsigned canon(unsigned u) { return u < 0xFFFFFFFEu ? u : 0xFFFFFFFEu; }
DEV v4f mfma(v4u a, v4u b, v4f c) { return __builtin_amdgcn_mfma_f32_16x16x32_bf16(__builtin_bit_cast(v8bf, a), __builtin_bit_cast(v8bf, b), c, 0, 0, 0); }
DEV v4f smf(v4u a, v8u b, v4f c, int idx) { return __builtin_amdgcn_smfmac_f32_16x16x64_bf16(__builtin_bit_cast(v8bf, a), __builtin_bit_cast(v16bf, b), c, idx, 0, 0); }
DEV v2u trd(unsigned a) { return __builtin_bit_cast(v2u, __builtin_amdgcn_ds_read_tr16_b64_v4i16((LDSP(v4s))(size_t)a)); }
DEV v4u lr128(unsigned a) { return *(LDSP(v4u))(size_t)a; }
DEV v4f lr128f(unsigned a) { return *(LDSP(v4f))(size_t)a; }
DEV void lw128f(unsigned a, v4f v) { *(LDSP(v4f))(size_t)a = v; }
DEV void lw128(unsigned a, v4u v) { *(LDSP(v4u))(size_t)a = v; }
DEV void lw64(unsigned a, v2u v) { *(LDSP(v2u))(size_t)a = v; }
DEV v4u lo4(v8u v) { return __builtin_shufflevector(v, v, 0, 1, 2, 3); }
DEV v4u hi4(v8u v) { return __builtin_shufflevector(v, v, 4, 5, 6, 7); }
DEV void bar() { asm volatile("s_waitcnt lgkmcnt(0)\n\ts_barrier" ::: "memory"); }
template <int N> DEV void waitvm() { asm volatile("s_waitcnt vmcnt(%0)" ::"i"(N) : "memory"); }
DEV void fence_c() { asm volatile("" ::: "memory"); }
DEV void glds(const void* g, unsigned dst) {
    unsigned keep;
    asm volatile("s_mov_b32 %0, m0\n\ts_mov_b32 m0, %2\n\ts_nop 0\n\tglobal_load_lds_dwordx4 %1, off\n\ts_mov_b32 m0, %0" : "=&s"(keep) : "v"(g), "s"(dst) : "memory");
}
DEV void glds_sc1(const void* g, unsigned dst) {
    unsigned keep;
    asm volatile("s_mov_b32 %0, m0\n\ts_mov_b32 m0, %2\n\ts_nop 0\n\tglobal_load_lds_dwordx4 %1, off sc1\n\ts_mov_b32 m0, %0" : "=&s"(keep) : "v"(g), "s"(dst) : "memory");
}
DEV rsrc_t mkr(const void* p, unsigned bytes) { return __builtin_amdgcn_make_buffer_rsrc((void*)p, 0, bytes, 0x00020000); }
DEV v4u ld128(rsrc_t r, unsigned vo, unsigned so) { return __builtin_amdgcn_raw_buffer_load_b128(r, vo, so, 16); }
DEV v2u ld64(rsrc_t r, unsigned vo, unsigned so) { return __builtin_amdgcn_raw_buffer_load_b64(r, vo, so, 16); }
DEV unsigned ld32(rsrc_t r, unsigned vo, unsigned so) { return __builtin_amdgcn_raw_buffer_load_b32(r, vo, so, 16); }
DEV void st128(rsrc_t r, unsigned vo, unsigned so, v4u v) { __builtin_amdgcn_raw_buffer_store_b128(v, r, vo, so, 16); }
DEV void st64(rsrc_t r, unsigned vo, unsigned so, v2u v) { __builtin_amdgcn_raw_buffer_store_b64(v, r, vo, so, 16); }
DEV void st32(rsrc_t r, unsigned vo, unsigned so, unsigned v) { __builtin_amdgcn_raw_buffer_store_b32(v, r, vo, so, 16); }
DEV unsigned umax(unsigned a, unsigned b) { return a > b ? a : b; }
DEV unsigned mx4(v4u v) { return umax(umax(v.x, v.y), umax(v.z, v.w)); }
DEV unsigned hmap(unsigned p) { return ((p >> 13) << 14) + (p & 8191u); }
DEV float xmax(float v) {
    auto a = __builtin_amdgcn_permlane16_swap(__float_as_uint(v), __float_as_uint(v), false, false);
    v = fmaxf(__uint_as_float(a[0]), __uint_as_float(a[1]));
    auto b = __builtin_amdgcn_permlane32_swap(__float_as_uint(v), __float_as_uint(v), false, false);
    return fmaxf(__uint_as_float(b[0]), __uint_as_float(b[1]));
}
DEV float xsum(float v) {
    auto a = __builtin_amdgcn_permlane16_swap(__float_as_uint(v), __float_as_uint(v), false, false);
    v = __uint_as_float(a[0]) + __uint_as_float(a[1]);
    auto b = __builtin_amdgcn_permlane32_swap(__float_as_uint(v), __float_as_uint(v), false, false);
    return __uint_as_float(b[0]) + __uint_as_float(b[1]);
}
DEV float hsum2(float a, float b) {
    auto r = __builtin_amdgcn_permlane32_swap(__float_as_uint(a), __float_as_uint(b), false, false);
    return __uint_as_float(r[0]) + __uint_as_float(r[1]);
}
DEV v2u hsum_pk(v4f a, v4f b) {
    v2u r; r.x = pk(hsum2(a[0], b[0]), hsum2(a[1], b[1])); r.y = pk(hsum2(a[2], b[2]), hsum2(a[3], b[3])); return r;
}

struct Params {
    const char* x; const char* mem0; const char* Wq; const char* Wk; const char* Wv; const char* Wo;
    const char* Wg; const char* Wu; const char* Wd; char* out; char* ws; unsigned long long mask;
};

__global__ void __launch_bounds__(256) prep_kernel(Params P) {
    __shared__ __attribute__((aligned(16))) char sh[192 * 160];
    const int b = blockIdx.x, tid = threadIdx.x, lane = tid & 63, w = __builtin_amdgcn_readfirstlane(tid >> 6);
    const int i16 = lane & 15, g = lane >> 4;
    const int h = b >> 3, ob = b & 7;
    char* ws = P.ws;
    {
        v4u t[8];
#pragma unroll
        for (int i = 0; i < 8; ++i) { int u = i * 256 + tid; u = u < 1920 ? u : 1919; int r = u / 10, c = u - r * 10; t[i] = *(const v4u*)(P.Wo + ((size_t)(h * 192 + r) * 640 + ob * 80) * 2 + c * 16); }
#pragma unroll
        for (int i = 0; i < 8; ++i) { int u = i * 256 + tid; if (u < 1920) *(v4u*)(sh + u * 16) = t[i]; }
    }
    {
        v4u xa[2][2];
        const int lc = lane < 40 ? lane : 39;
#pragma unroll
        for (int r = 0; r < 2; ++r) { const char* p = P.x + (size_t)(b * 8 + w * 2 + r) * 1280 + lc * 32; xa[r][0] = *(const v4u*)p; xa[r][1] = *(const v4u*)(p + 16); }
#pragma unroll
        for (int r = 0; r < 2; ++r) {
            float f[16];
#pragma unroll
            for (int k = 0; k < 4; ++k) { f[2 * k] = blo(xa[r][0][k]); f[2 * k + 1] = bhi(xa[r][0][k]); f[8 + 2 * k] = blo(xa[r][1][k]); f[9 + 2 * k] = bhi(xa[r][1][k]); }
            float s = 0;
#pragma unroll
            for (int k = 0; k < 16; ++k) s += f[k];
            if (lane >= 40) s = 0;
#pragma unroll
            for (int o = 32; o; o >>= 1) s += __shfl_xor(s, o);
            const float mean = s * (1.f / 640.f);
            float v = 0;
#pragma unroll
            for (int k = 0; k < 16; ++k) { float d = f[k] - mean; v += d * d; }
            if (lane >= 40) v = 0;
#pragma unroll
            for (int o = 32; o; o >>= 1) v += __shfl_xor(v, o);
            v *= (1.f / 640.f);
            const float mb = rbf(mean), den = rbf(sqrtf(rbf(rbf(v) + 1e-5f)));
            unsigned d[8];
#pragma unroll
            for (int k = 0; k < 8; ++k) d[k] = pk(rbf(f[2 * k] - mb) / den, rbf(f[2 * k + 1] - mb) / den);
            const int tok = b * 8 + w * 2 + r;
            if (lane < 40) {
                char* dst = ws + OFF_XNF + ((size_t)((tok >> 4) * 20 + (lane >> 1)) * 64 + (lane & 1) * 32 + (tok & 15)) * 16;
                *(v4u*)dst = v4u{d[0], d[4], d[1], d[5]};
                *(v4u*)(dst + 256) = v4u{d[2], d[6], d[3], d[7]};
            }
        }
    }
    {
        constexpr int NU = (int)(POISON_END / 16 / 256);
        static_assert(POISON_END % (16 * 256) == 0, "");
        const v4u ones = {0xFFFFFFFFu, 0xFFFFFFFFu, 0xFFFFFFFFu, 0xFFFFFFFFu};
        for (int u = tid; u < NU; u += 256) {
            const unsigned a = (unsigned)(b * NU + u) * 16u;
            const bool m0 = a < 16384u || (a < 32768u && (a & 8191u) < 2048u);
            if (!m0) *(v4u*)(ws + a) = ones;
        }
        if (b == 1) for (int u = tid; u < (int)((FLAG_END - OFF_FLAG) / 16); u += 256) *(v4u*)(ws + OFF_FLAG + u * 16) = v4u{0, 0, 0, 0};
        if (b == 2) for (int u = tid; u < 640; u += 256) {
            v4u v = *(const v4u*)(P.mem0 + (u & 7) * 1280 + (u >> 3) * 16);
            v.x = canon(v.x); v.y = canon(v.y); v.z = canon(v.z); v.w = canon(v.w);
            const unsigned a = hmap(u * 16);
            *(v4u*)(ws + OFF_HAND + a) = v; *(v4u*)(ws + OFF_HAND + a + 8192) = v;
        }
    }
    __syncthreads();
    const unsigned sb = (unsigned)(size_t)(LDSP(char))sh;
    v4u B[5][6];
    {
        const unsigned tb = sb + (8 * g + (i16 >> 2)) * 160 + (i16 & 3) * 8;
#pragma unroll
        for (int ot = 0; ot < 5; ++ot)
#pragma unroll
            for (int n = 0; n < 6; ++n) { v2u r0 = trd(tb + n * 32 * 160 + ot * 32), r1 = trd(tb + (n * 32 + 4) * 160 + ot * 32); B[ot][n] = v4u{r0.x, r0.y, r1.x, r1.y}; }
    }
    const int drow = 16 * (i16 >> 3) + 4 * ((i16 >> 2) & 1) + 8 * ((i16 >> 1) & 1) + (i16 & 1);
    const char* ap = P.Wv + (size_t)drow * 12288 + h * 384 + g * 16;
    const int wv = h >> 3, tv = (h >> 1) & 3, a = h & 1;
    v4u A[2][2][6];
    auto lda = [&](int buf, int kh) {
#pragma unroll
        for (int hf = 0; hf < 2; ++hf)
#pragma unroll
            for (int n = 0; n < 6; ++n) A[buf][hf][n] = *(const v4u*)(ap + (size_t)(kh * 32 + 2 * hf) * 12288 + n * 64);
    };
    lda(0, w * 5);
#pragma unroll
    for (int k = 0; k < 5; ++k) {
        const int kh = w * 5 + k;
        if (k < 4) lda((k + 1) & 1, kh + 1);
#pragma unroll
        for (int ot = 0; ot < 5; ++ot) {
            v4f c0 = {0, 0, 0, 0}, c1 = {0, 0, 0, 0};
#pragma unroll
            for (int n = 0; n < 6; ++n) { c0 = mfma(A[k & 1][0][n], B[ot][n], c0); c1 = mfma(A[k & 1][1][n], B[ot][n], c1); }
            const int j = ob * 10 + ot * 2 + (i16 >> 3);
            char* dst = ws + OFF_UP + ((size_t)(((j * 4 + wv) * 4 + tv) * 20 + kh) * 64 + g * 16 + a * 8 + (i16 & 7)) * 16;
            *(v4u*)dst = v4u{pk(c0[0], c0[1]), pk(c0[2], c0[3]), pk(c1[0], c1[1]), pk(c1[2], c1[3])};
        }
    }
}

template <int ROWS, int SEGU, int NI>
DEV void stage(const char* p0, const char* p1, unsigned ldb, int row0, unsigned dst, int w, int lane) {
    constexpr int UPR = 2 * SEGU + 2;
#pragma unroll
    for (int i = 0; i < NI; ++i) {
        const int u = (i * 4 + w) * 64 + lane;
        int row = u / UPR; const int cu = u - row * UPR; row = row < ROWS ? row : ROWS - 1;
        const char* src = cu < SEGU ? p0 + cu * 16 : (cu < 2 * SEGU ? p1 + (cu - SEGU) * 16 : p0);
        glds(src + (size_t)(row0 + row) * ldb, dst + (i * 4 + w) * 1024);
    }
}
DEV void wait_flags(rsrc_t r, unsigned vo, bool act, unsigned& dead) {
    for (unsigned it = 0;; ++it) {
        fence_c();
        unsigned v = ld32(r, vo, 0);
        if (__all(!act || v != 0u)) break;
        if (dead || it > SPIN_LIMIT) { dead = 1; break; }
        __builtin_amdgcn_s_sleep(2);
    }
}
DEV void poll_m(rsrc_t r, unsigned vo, unsigned so, v4u (&a)[10], unsigned& dead) {
    for (unsigned it = 0;; ++it) {
        fence_c();
#pragma unroll
        for (int c = 0; c < 10; ++c) a[c] = ld128(r, vo + c * 1024 + (c >= 8 ? 8192 : 0), so);
        unsigned m = 0;
#pragma unroll
        for (int c = 0; c < 10; ++c) m = umax(m, mx4(a[c]));
        if (__all(m != 0xFFFFFFFFu)) break;
        if (dead || it > SPIN_LIMIT) { dead = 1; break; }
    }
}
DEV void poll_p(rsrc_t r, const unsigned (&vo)[6], unsigned so, v4u (&p)[5], unsigned& dead) {
    for (unsigned it = 0;; ++it) {
        fence_c();
#pragma unroll
        for (int t = 0; t < 4; ++t) p[t] = ld128(r, vo[t], so);
        v2u m0 = ld64(r, vo[4], so), m1 = ld64(r, vo[5], so);
        p[4] = v4u{m0.x, m0.y, m1.x, m1.y};
        unsigned m = 0;
#pragma unroll
        for (int t = 0; t < 5; ++t) m = umax(m, mx4(p[t]));
        if (__all(m != 0xFFFFFFFFu)) break;
        if (dead || it > SPIN_LIMIT) { dead = 1; break; }
    }
}

DEV void score_role(const Params& P, const int h, const unsigned lb, const int w, const int lane, const unsigned copy_off) {
    const int i16 = lane & 15, g = lane >> 4;
    char* ws = P.ws;
    constexpr unsigned SLOT = 53248, L_QKX = 2 * SLOT, L_X = 131072;
    const char* pq = P.Wq + h * 384; const char* pkk = P.Wk + h * 384;
    v8u W[6][8];
    const unsigned trb = lb + (16 * (g >> 1) + 4 * (g & 1) + (i16 >> 2)) * 800 + (i16 & 3) * 8 + 96 * w;
    auto unit = [&](unsigned slot_off, int t) {
        const unsigned o = trb + slot_off + (t < 3 ? 0 : 384) + 32 * (t % 3);
        v2u r00 = trd(o), r01 = trd(o + 8 * 800), r10 = trd(o + 32 * 800), r11 = trd(o + 40 * 800);
        return v8u{r00.x, r01.x, r00.y, r01.y, r10.x, r11.x, r10.y, r11.y};
    };
    sfor<0, 3>([&](auto C) { stage<64, 24, 13>(pq, pkk, 12288, C.v * 64, lb + ((C.v + 1) % 3) * SLOT, w, lane); });
    sfor<0, 8>([&](auto C) {
        constexpr int c = C.v;
        waitvm<26>(); bar();
#pragma unroll
        for (int t = 0; t < 6; ++t) W[t][c] = unit(((c + 1) % 3) * SLOT, t);
        bar();
        if constexpr (c + 3 < 10) stage<64, 24, 13>(pq, pkk, 12288, (c + 3) * 64, lb + ((c + 4) % 3) * SLOT, w, lane);
    });
    unsigned dead = 0;
    const rsrc_t rh = mkr(ws + OFF_HAND, 64 * 65536), rpx = mkr(ws + OFF_PX, 64 * 32 * 2560), rf = mkr(ws + OFF_FLAG, 64 * 192 * 4);
    const unsigned fvo = ((lane < 3 ? 0 : 93) + 3 * h + lane) * 4;
    const char* qkx = ws + OFF_QKX + (size_t)h * 24576 + w * 6144 + lane * 16;
    auto dma_qkx = [&](int s) {
#pragma unroll
        for (int i = 0; i < 6; ++i) glds_sc1(qkx + (size_t)s * (32 * 24576) + i * 1024, lb + L_QKX + w * 6144 + i * 1024);
    };
    wait_flags(rf, lane < 6 ? fvo : 0, lane < 6, dead);
    waitvm<0>(); bar();
    dma_qkx(0);
    const int idx = (lane & 8) ? 0xEEEE : 0x4444;
    const unsigned mvo = lane * 16 + copy_off;
    const unsigned pb = 10240 + h * 640;
    const unsigned pvx = hmap(pb + (g * 8 + (i16 & 7)) * 16), pvm = hmap(pb + 512 + ((g & 1) * 8 + (i16 & 7)) * 8);
    const unsigned xtr = lb + L_X + (4 * g + (i16 >> 2)) * 32 + (i16 & 3) * 8;
    constexpr float SC = 0.07216878364870322f, L2E = 1.4426950408889634f;
    for (int s = 0; s < 64; ++s) {
        v4u a[10];
        poll_m(rh, mvo, s * 65536, a, dead);
        const unsigned xo = (s & 1) * 6144;
#pragma unroll
        for (int p = 0; p < 3; ++p) {
            v4f cq = {0, 0, 0, 0}, ck = {0, 0, 0, 0};
#pragma unroll
            for (int c = 0; c < 8; ++c) { cq = smf(a[c], W[p][c], cq, idx); ck = smf(a[c], W[3 + p][c], ck, idx); }
#pragma unroll
            for (int c = 8; c < 10; ++c) { cq = smf(a[c], unit((c - 8) * SLOT, p), cq, idx); ck = smf(a[c], unit((c - 8) * SLOT, 3 + p), ck, idx); }
            lw64(lb + L_X + xo + (48 * w + 16 * p + i16) * 32 + g * 8, hsum_pk(cq, ck));
        }
        bar();
        if (w < 3) {
            v4u X[6];
#pragma unroll
            for (int n = 0; n < 6; ++n) { v2u r0 = trd(xtr + xo + n * 1024), r1 = trd(xtr + xo + n * 1024 + 512); X[n] = v4u{r0.x, r0.y, r1.x, r1.y}; }
            const unsigned kb = lb + L_QKX + 12288 + lane * 16;
            if (w == 0) {
                if (s < 63) {
                    v4f tm = {0, 0, 0, 0}, t0 = {0, 0, 0, 0}, t1 = {0, 0, 0, 0};
#pragma unroll
                    for (int n = 0; n < 6; ++n) { t0 = mfma(lr128(kb + n * 1024), X[n], t0); t1 = mfma(lr128(kb + 6144 + n * 1024), X[n], t1); tm = mfma(X[n], X[n], tm); }
                    if (g < 2) tm = v4f{-INFINITY, -INFINITY, -INFINITY, -INFINITY};
                    float mx = fmaxf(fmaxf(fmaxf(t0[0], t0[1]), fmaxf(t0[2], t0[3])), fmaxf(fmaxf(t1[0], t1[1]), fmaxf(t1[2], t1[3])));
                    mx = fmaxf(mx, fmaxf(fmaxf(tm[0], tm[1]), fmaxf(tm[2], tm[3])));
                    mx = xmax(mx) * (SC * L2E);
                    float sm = 0;
#pragma unroll
                    for (int i = 0; i < 4; ++i) {
                        t0[i] = __builtin_amdgcn_exp2f(t0[i] * (SC * L2E) - mx); t1[i] = __builtin_amdgcn_exp2f(t1[i] * (SC * L2E) - mx); tm[i] = __builtin_amdgcn_exp2f(tm[i] * (SC * L2E) - mx);
                        sm += t0[i] + t1[i] + tm[i];
                    }
                    const float inv = __builtin_amdgcn_rcpf(xsum(sm));
                    v4u px = {canon(pk(t0[0] * inv, t0[1] * inv)), canon(pk(t0[2] * inv, t0[3] * inv)), canon(pk(t1[0] * inv, t1[1] * inv)), canon(pk(t1[2] * inv, t1[3] * inv))};
                    v2u pm = {canon(pk(tm[0] * inv, tm[1] * inv)), canon(pk(tm[2] * inv, tm[3] * inv))};
                    if (i16 < 8) {
                        st128(rh, pvx, s * 65536, px); st128(rh, pvx + 8192, s * 65536, px);
                        if (g >= 2) { st64(rh, pvm, s * 65536, pm); st64(rh, pvm + 8192, s * 65536, pm); }
                    }
                }
            } else {
                const int qt = w - 1;
                const unsigned qb = lb + L_QKX + qt * 6144 + lane * 16;
                v4f tm = {0, 0, 0, 0}, t0 = {0, 0, 0, 0}, t1 = {0, 0, 0, 0};
#pragma unroll
                for (int n = 0; n < 6; ++n) { v4u q = lr128(qb + n * 1024); t0 = mfma(lr128(kb + n * 1024), q, t0); t1 = mfma(lr128(kb + 6144 + n * 1024), q, t1); tm = mfma(X[n], q, tm); }
                const int qi = 16 * qt + i16;
                float mx = -INFINITY;
#pragma unroll
                for (int i = 0; i < 4; ++i) {
                    t0[i] = (4 * g + i <= qi) ? rbf(rbf(t0[i]) * SC) : -1e9f;
                    t1[i] = (16 + 4 * g + i <= qi) ? rbf(rbf(t1[i]) * SC) : -1e9f;
                    tm[i] = (g >= 2) ? rbf(rbf(tm[i]) * SC) : -INFINITY;
                    mx = fmaxf(mx, fmaxf(fmaxf(t0[i], t1[i]), tm[i]));
                }
                mx = xmax(mx);
                float sm = 0;
#pragma unroll
                for (int i = 0; i < 4; ++i) {
                    t0[i] = __builtin_amdgcn_exp2f((t0[i] - mx) * L2E); t1[i] = __builtin_amdgcn_exp2f((t1[i] - mx) * L2E); tm[i] = __builtin_amdgcn_exp2f((tm[i] - mx) * L2E);
                    sm += t0[i] + t1[i] + tm[i];
                }
                const float inv = 1.f / xsum(sm);
                v4u px = {canon(pk(t0[0] * inv, t0[1] * inv)), canon(pk(t0[2] * inv, t0[3] * inv)), canon(pk(t1[0] * inv, t1[1] * inv)), canon(pk(t1[2] * inv, t1[3] * inv))};
                v2u pm = {canon(pk(tm[0] * inv, tm[1] * inv)), canon(pk(tm[2] * inv, tm[3] * inv))};
                const unsigned so = (s * 32 + h) * 2560, rgo = (2 * qt + (i16 >> 3)) * 640;
                st128(rpx, rgo + (g * 8 + (i16 & 7)) * 16, so, px);
                if (g >= 2) st64(rpx, rgo + 512 + ((g & 1) * 8 + (i16 & 7)) * 8, so, pm);
            }
        } else if (s < 63) {
            wait_flags(rf, (lane < 6 ? fvo : 0) + (s + 1) * 768, lane < 6, dead);
        }
        bar();
        if (s < 63) dma_qkx(s + 1);
    }
}

DEV void value_role(const Params& P, const int j, const unsigned lb, const int w, const int lane, const unsigned copy_off) {
    const int i16 = lane & 15, g = lane >> 4;
    char* ws = P.ws;
    constexpr unsigned L_XN = 0, L_RED = 81920, L_RED2 = L_RED + 2048;
    const char* xnf = ws + OFF_XNF + w * 10240 + lane * 16;
    auto dma_xn = [&](int s) {
#pragma unroll
        for (int i = 0; i < 10; ++i) glds(xnf + (size_t)s * 40960 + i * 1024, lb + L_XN + (s & 1) * 40960 + w * 10240 + i * 1024);
    };
    dma_xn(0); dma_xn(1);
    v8u U[4][10];
    {
        const char* up = ws + OFF_UP + (size_t)(j * 4 + w) * 81920 + lane * 16;
#pragma unroll
        for (int t = 0; t < 4; ++t)
#pragma unroll
            for (int c = 0; c < 10; ++c) {
                v4u l = *(const v4u*)(up + (t * 20 + 2 * c) * 1024), hh = *(const v4u*)(up + (t * 20 + 2 * c + 1) * 1024);
                U[t][c] = __builtin_shufflevector(l, hh, 0, 1, 2, 3, 4, 5, 6, 7);
            }
    }
    auto zx_tile = [&](int s, int tt, v2u (&z)[4]) {
        const unsigned ab = lb + L_XN + (s & 1) * 40960 + tt * 20480 + lane * 16;
        v4f acc[4];
#pragma unroll
        for (int t = 0; t < 4; ++t) acc[t] = v4f{0, 0, 0, 0};
#pragma unroll
        for (int c = 0; c < 10; ++c) {
            v4u a0 = lr128(ab + c * 2048), a1 = lr128(ab + c * 2048 + 1024);
#pragma unroll
            for (int t = 0; t < 4; ++t) acc[t] = mfma(a0, lo4(U[t][c]), acc[t]);
#pragma unroll
            for (int t = 0; t < 4; ++t) acc[t] = mfma(a1, hi4(U[t][c]), acc[t]);
        }
#pragma unroll
        for (int t = 0; t < 4; ++t) z[t] = v2u{pk(acc[t][0], acc[t][1]), pk(acc[t][2], acc[t][3])};
    };
    unsigned dead = 0;
    const rsrc_t rh = mkr(ws + OFF_HAND, 64 * 65536), rpx = mkr(ws + OFF_PX, 64 * 32 * 2560), rff = mkr(ws + OFF_FFN, 2048 * 1280);
    const int idx = (lane & 8) ? 0xEEEE : 0x4444;
    const unsigned mvo = lane * 16 + copy_off;
    const int ap = i16 >> 3, r8 = i16 & 7;
    unsigned pvo[6], xvo[6];
#pragma unroll
    for (int t = 0; t < 4; ++t) { const int hd = 8 * w + 2 * t + ap; pvo[t] = hmap(10240 + hd * 640 + (g * 8 + r8) * 16) + copy_off; xvo[t] = hd * 2560 + (g * 8 + r8) * 16; }
#pragma unroll
    for (int q = 0; q < 2; ++q) { const int hd = 8 * w + 4 * q + 2 * (g >> 1) + ap; pvo[4 + q] = hmap(10240 + hd * 640 + 512 + ((g & 1) * 8 + r8) * 8) + copy_off; xvo[4 + q] = hd * 2560 + 512 + ((g & 1) * 8 + r8) * 8; }
    const bool valid = ap == (g >> 1);
    const unsigned redw = ((w * 2 + ap) * 16 + r8 * 2 + (g & 1)) * 16;
    const unsigned mso = hmap(j * 128 + (lane & 15) * 8) + ((lane >> 4) & 1) * 8192;
    v2u zc0[4], zc1[4], zn0[4], zn1[4];
    v4u zp[4], zmp;
    waitvm<0>(); bar();
    zx_tile(0, 0, zc0); zx_tile(0, 1, zc1);
    auto out_x = [&](int s) {
        const unsigned tok = 32 * s + 8 * w + (i16 >> 1);
        const unsigned oo = tok * 1280 + j * 16 + (i16 & 1) * 8;
        v2u xv = *(const v2u*)(P.x + oo);
#pragma unroll
        for (int rg = 0; rg < 4; ++rg) {
            v4u p[5];
            poll_p(rpx, xvo, s * 32 * 2560 + rg * 640, p, dead);
            v4f acc = mfma(zmp, p[4], v4f{0, 0, 0, 0});
#pragma unroll
            for (int t = 0; t < 4; ++t) acc = mfma(zp[t], p[t], acc);
            if (valid) lw128f(lb + L_RED2 + rg * 2048 + redw, acc);
        }
        v2u fv;
        for (unsigned it = 0;; ++it) {
            fence_c();
            fv = ld64(rff, oo, 0);
            if (__all(umax(fv.x, fv.y) != 0xFFFFFFFFu)) break;
            if (dead || it > SPIN_LIMIT) { dead = 1; break; }
        }
        bar();
        v4f sum = lr128f(lb + L_RED2 + w * 2048 + i16 * 16);
#pragma unroll
        for (int q = 1; q < 8; ++q) sum += lr128f(lb + L_RED2 + w * 2048 + q * 256 + i16 * 16);
        v2u o;
        o.x = pk(rbf(blo(xv.x) + rbf(sum[0])) + blo(fv.x), rbf(bhi(xv.x) + rbf(sum[1])) + bhi(fv.x));
        o.y = pk(rbf(blo(xv.y) + rbf(sum[2])) + blo(fv.y), rbf(bhi(xv.y) + rbf(sum[3])) + bhi(fv.y));
        if (lane < 16) *(v2u*)(P.out + oo) = o;
    };
    for (int s = 0; s < 64; ++s) {
        v4u zm;
        {
            v4u a[10];
            poll_m(rh, mvo, s * 65536, a, dead);
            v4f am[4];
#pragma unroll
            for (int t = 0; t < 4; ++t) am[t] = v4f{0, 0, 0, 0};
#pragma unroll
            for (int c = 0; c < 10; ++c)
#pragma unroll
                for (int t = 0; t < 4; ++t) am[t] = smf(a[c], U[t][c], am[t], idx);
            v2u z01 = hsum_pk(am[0], am[1]), z23 = hsum_pk(am[2], am[3]);
            zm = v4u{z01.x, z01.y, z23.x, z23.y};
        }
        if (s > 0) out_x(s - 1); else bar();
        if (s + 2 < 64) dma_xn(s + 2);
        if (s < 63) {
            zx_tile(s + 1, 0, zn0);
            v4u p[5];
            poll_p(rh, pvo, s * 65536, p, dead);
            v4f acc = mfma(zm, p[4], v4f{0, 0, 0, 0}), acc2 = {0, 0, 0, 0};
            acc2 = mfma(v4u{zc0[0].x, zc0[0].y, zc1[0].x, zc1[0].y}, p[0], acc2);
            acc = mfma(v4u{zc0[1].x, zc0[1].y, zc1[1].x, zc1[1].y}, p[1], acc);
            acc2 = mfma(v4u{zc0[2].x, zc0[2].y, zc1[2].x, zc1[2].y}, p[2], acc2);
            acc = mfma(v4u{zc0[3].x, zc0[3].y, zc1[3].x, zc1[3].y}, p[3], acc);
            acc += acc2;
            if (valid) lw128f(lb + L_RED + redw, acc);
            bar();
            if (w == 0) {
                v4f sum = lr128f(lb + L_RED + i16 * 16);
#pragma unroll
                for (int q = 1; q < 8; ++q) sum += lr128f(lb + L_RED + q * 256 + i16 * 16);
                if (lane < 32) st64(rh, mso, (s + 1) * 65536, v2u{canon(pk(sum[0], sum[1])), canon(pk(sum[2], sum[3]))});
            }
            zx_tile(s + 1, 1, zn1);
        }
#pragma unroll
        for (int t = 0; t < 4; ++t) { zp[t] = v4u{zc0[t].x, zc0[t].y, zc1[t].x, zc1[t].y}; zc0[t] = zn0[t]; zc1[t] = zn1[t]; }
        zmp = zm;
    }
    bar();
    out_x(63);
}

template <bool FFN1>
DEV void xn_helper(const Params& P, const int hb, const unsigned lb, const int w, const int lane) {
    const int i16 = lane & 15, g = lane >> 4;
    char* ws = P.ws;
    constexpr unsigned SLOT = 36864;
    const char *p0, *p1; unsigned ldb;
    if constexpr (FFN1) { const int cb = hb % 12; p0 = P.Wg + cb * 256; p1 = P.Wu + cb * 256; ldb = 3072; }
    else { p0 = (hb < 24 ? P.Wq : P.Wk) + (hb % 24) * 512; p1 = p0 + 256; ldb = 12288; }
    v4u W[4][20];
    const unsigned trb = lb + (16 * (g >> 1) + 4 * (g & 1) + (i16 >> 2)) * 544 + (i16 & 3) * 8 + (FFN1 ? 64 * w : 128 * w);
    sfor<0, 4>([&](auto C) { stage<64, 16, 9>(p0, p1, ldb, C.v * 64, lb + C.v * SLOT, w, lane); });
    sfor<0, 10>([&](auto C) {
        constexpr int c = C.v;
        waitvm<(c <= 6 ? 27 : (9 - c) * 9)>(); bar();
#pragma unroll
        for (int t = 0; t < 4; ++t)
#pragma unroll
            for (int k = 0; k < 2; ++k) {
                const unsigned o = trb + (c % 4) * SLOT + (FFN1 ? (t >> 1) * 256 + (t & 1) * 32 : t * 32) + k * 32 * 544;
                v2u r0 = trd(o), r1 = trd(o + 8 * 544);
                W[t][2 * c + k] = v4u{r0.x, r1.x, r0.y, r1.y};
            }
        bar();
        if constexpr (c + 4 < 10) stage<64, 16, 9>(p0, p1, ldb, (c + 4) * 64, lb + (c % 4) * SLOT, w, lane);
    });
    const int s0 = FFN1 ? hb / 12 : 0, sstep = FFN1 ? 2 : 1;
    const char* xnf = ws + OFF_XNF + w * 10240 + lane * 16;
    auto dma_xn = [&](int s, int buf) {
#pragma unroll
        for (int i = 0; i < 10; ++i) glds(xnf + (size_t)s * 40960 + i * 1024, lb + buf * 40960 + w * 10240 + i * 1024);
    };
    const rsrc_t ro = FFN1 ? mkr(ws + OFF_HF, 128 * 48 * 1024) : mkr(ws + OFF_QKX, 64 * 32 * 24576);
    const rsrc_t rf = mkr(ws + OFF_FLAG, (unsigned)(FLAG_END - OFF_FLAG));
    unsigned ovo, fo, fstep;
    if constexpr (FFN1) { const int kf = (hb % 12) * 4 + w; ovo = kf * 1024 + lane * 16; fo = 64 * 192 * 4 + kf * 4; fstep = 256; }
    else { const int col = (hb % 24) * 256 + 64 * w, hd = col / 192, n0 = (col - hd * 192) >> 5; ovo = hd * 24576 + (hb < 24 ? 0 : 12288) + n0 * 1024 + lane * 16; fo = (hb * 4 + w) * 4; fstep = 768; }
    dma_xn(s0, 0);
    waitvm<0>();
    int buf = 0;
    for (int s = s0; s < 64; s += sstep, buf ^= 1) {
        bar();
        if (s + sstep < 64) dma_xn(s + sstep, buf ^ 1);
#pragma unroll
        for (int tt = 0; tt < 2; ++tt) {
            const unsigned bb = lb + buf * 40960 + tt * 20480 + lane * 16;
            v4f acc[4];
#pragma unroll
            for (int t = 0; t < 4; ++t) acc[t] = v4f{0, 0, 0, 0};
#pragma unroll
            for (int kh = 0; kh < 20; ++kh) {
                v4u bfr = lr128(bb + kh * 1024);
#pragma unroll
                for (int t = 0; t < 4; ++t) acc[t] = mfma(W[t][kh], bfr, acc[t]);
            }
            if (tt == 0) {
                waitvm<0>();
                if (s > s0 && lane == 0) st32(rf, fo, (s - sstep) * fstep, 1u);
            }
            if constexpr (FFN1) {
                float hv[8];
#pragma unroll
                for (int t = 0; t < 2; ++t)
#pragma unroll
                    for (int i = 0; i < 4; ++i) { float gt = rbf(acc[t][i]), up = rbf(acc[2 + t][i]); gt = gt * (gt > 0.f ? 1.f : 0.f); hv[4 * t + i] = gt * up; }
                st128(ro, ovo, (2 * s + tt) * 49152, v4u{pk(hv[0], hv[1]), pk(hv[2], hv[3]), pk(hv[4], hv[5]), pk(hv[6], hv[7])});
            } else {
#pragma unroll
                for (int q = 0; q < 2; ++q)
                    st128(ro, ovo + tt * 6144 + q * 1024, s * (32 * 24576), v4u{pk(acc[2 * q][0], acc[2 * q][1]), pk(acc[2 * q][2], acc[2 * q][3]), pk(acc[2 * q + 1][0], acc[2 * q + 1][1]), pk(acc[2 * q + 1][2], acc[2 * q + 1][3])});
            }
        }
    }
    waitvm<0>();
    if (lane == 0) st32(rf, fo, (64 - sstep + s0) * fstep, 1u);
}

DEV void ffn2_role(const Params& P, const int fb, const unsigned lb, const int w, const int lane) {
    const int i16 = lane & 15, g = lane >> 4;
    char* ws = P.ws;
    constexpr unsigned SLOT = 36864;
    const int grp = fb / 5, db = fb % 5;
    const char* p0 = P.Wd + db * 256; const char* p1 = p0 + 128;
    v4u W[2][48];
    const unsigned trb = lb + (4 * g + (i16 >> 2)) * 288 + (i16 & 3) * 8 + 64 * w;
    sfor<0, 4>([&](auto C) { stage<128, 8, 9>(p0, p1, 1280, C.v * 128, lb + C.v * SLOT, w, lane); });
    sfor<0, 12>([&](auto C) {
        constexpr int c = C.v;
        waitvm<(c <= 8 ? 27 : (11 - c) * 9)>(); bar();
#pragma unroll
        for (int t = 0; t < 2; ++t)
#pragma unroll
            for (int k = 0; k < 4; ++k) {
                const unsigned o = trb + (c % 4) * SLOT + t * 32 + k * 32 * 288;
                v2u r0 = trd(o), r1 = trd(o + 16 * 288);
                W[t][4 * c + k] = v4u{r0.x, r0.y, r1.x, r1.y};
            }
        bar();
        if constexpr (c + 4 < 12) stage<128, 8, 9>(p0, p1, 1280, (c + 4) * 128, lb + (c % 4) * SLOT, w, lane);
    });
    unsigned dead = 0;
    const rsrc_t rf = mkr(ws + OFF_FLAGH, 64 * 256), ro = mkr(ws + OFF_FFN, 2048 * 1280);
    const char* hf = ws + OFF_HF + w * 24576 + lane * 16;
    for (int s = grp; s < 64; s += 8) {
        wait_flags(rf, s * 256 + lane * 4, lane < 48, dead);
#pragma unroll
        for (int i = 0; i < 24; ++i) glds_sc1(hf + (size_t)s * 98304 + i * 1024, lb + w * 24576 + i * 1024);
        waitvm<0>(); bar();
#pragma unroll
        for (int tt = 0; tt < 2; ++tt) {
            const unsigned bb = lb + tt * 49152 + lane * 16;
            v4f acc[2] = {{0, 0, 0, 0}, {0, 0, 0, 0}};
#pragma unroll
            for (int kf = 0; kf < 48; ++kf) {
                v4u bfr = lr128(bb + kf * 1024);
                acc[0] = mfma(W[0][kf], bfr, acc[0]); acc[1] = mfma(W[1][kf], bfr, acc[1]);
            }
#pragma unroll
            for (int t = 0; t < 2; ++t)
                st64(ro, (32 * s + 16 * tt + i16) * 1280 + (db * 128 + w * 32 + t * 16 + 4 * g) * 2, 0, v2u{canon(pk(acc[t][0], acc[t][1])), canon(pk(acc[t][2], acc[t][3]))});
        }
        bar();
    }
}

__global__ void __launch_bounds__(256, 1) main_kernel(Params P) {
    extern __shared__ __attribute__((aligned(16))) char lds[];
    const int b = blockIdx.x, tid = threadIdx.x, lane = tid & 63, w = __builtin_amdgcn_readfirstlane(tid >> 6);
    const unsigned lb = (unsigned)(size_t)(LDSP(char))lds;
    const unsigned xcc = __builtin_amdgcn_s_getreg((20) | (0 << 6) | (3 << 11));
    const unsigned copy_off = ((xcc >> 2) & 1) * 8192;
    if (b < 32) { if (P.mask & 1) score_role(P, b, lb, w, lane, copy_off); }
    else if (b < 112) { if (P.mask & 1) value_role(P, b - 32, lb, w, lane, copy_off); }
    else if (b < 160) { if (P.mask & 2) xn_helper<false>(P, b - 112, lb, w, lane); }
    else if (b < 184) { if (P.mask & 2) xn_helper<true>(P, b - 160, lb, w, lane); }
    else if (b < 224) { if (P.mask & 2) ffn2_role(P, b - 184, lb, w, lane); }
}

extern "C" void kernel_launch(void* const* d_in, const int* in_sizes, int n_in, void* d_out, int out_size, void* d_ws, size_t ws_size, hipStream_t stream) {
    Params P;
    P.x = (const char*)d_in[0]; P.mem0 = (const char*)d_in[1]; P.Wq = (const char*)d_in[2]; P.Wk = (const char*)d_in[3]; P.Wv = (const char*)d_in[4]; P.Wo = (const char*)d_in[5];
    P.Wg = (const char*)d_in[8]; P.Wu = (const char*)d_in[9]; P.Wd = (const char*)d_in[10]; P.out = (char*)d_out;
    P.ws = (char*)(((uintptr_t)d_ws + ((1u << 21) - 1)) & ~(uintptr_t)((1u << 21) - 1));
    P.mask = 3;
    (void)hipFuncSetAttribute((const void*)main_kernel, hipFuncAttributeMaxDynamicSharedMemorySize, LDS_BYTES);
    prep_kernel<<<256, 256, 0, stream>>>(P);
    main_kernel<<<224, 256, LDS_BYTES, stream>>>(P);
}
```

```cpp
#include <hip/hip_runtime.h>
#include <stdint.h>
#define C1 18
#define D2 59
#define V1A 30
#define V1B 2
#define Q3 1
#define E4 2


typedef __bf16 v8bf __attribute__((ext_vector_type(8)));
typedef __bf16 v16bf __attribute__((ext_vector_type(16)));
typedef __bf16 v2bf __attribute__((ext_vector_type(2)));
typedef float v4f __attribute__((ext_vector_type(4)));
typedef float v2f __attribute__((ext_vector_type(2)));
typedef unsigned v4u __attribute__((ext_vector_type(4)));
typedef unsigned v2u __attribute__((ext_vector_type(2)));
typedef unsigned v8u __attribute__((ext_vector_type(8)));
typedef unsigned v16u __attribute__((ext_vector_type(16)));
typedef short v4s __attribute__((ext_vector_type(4)));
#define DEV __device__ __forceinline__
#define LDSP(T) __attribute__((address_space(3))) T*
typedef __amdgpu_buffer_rsrc_t rsrc_t;

constexpr size_t OFF_HAND = 0;
constexpr size_t OFF_PX = OFF_HAND + 64 * 65536;
constexpr size_t OFF_FFN = OFF_PX + 64 * 32 * 2560;
constexpr size_t OFF_FLAG = OFF_FFN + 2048 * 640 * 2;
constexpr size_t OFF_FLAGH = OFF_FLAG + 64 * 192 * 4;
constexpr size_t OFF_FLAGX = OFF_FLAGH + 64 * 64 * 4;
constexpr size_t OFF_FLAGF = OFF_FLAGX + 21504;
constexpr size_t OFF_INIT = OFF_FLAGF + 64 * 128;
constexpr size_t OFF_VOTE = OFF_INIT + 1024;
constexpr size_t FLAG_END = OFF_VOTE + 1024;
constexpr size_t OFF_XNF = FLAG_END + 40960;
constexpr size_t OFF_UP = OFF_XNF + 2048 * 640 * 2;
constexpr size_t OFF_QKX = OFF_UP + (size_t)80 * 16 * 20 * 1024 + 32 * 24576;
constexpr size_t OFF_HF = OFF_QKX + (size_t)64 * 32 * 24576;
constexpr size_t OFF_Z = OFF_HF + (size_t)128 * 48 * 1024;
constexpr size_t WS_END = OFF_Z + (size_t)64 * 1638400;
constexpr unsigned QS = 32 * 24576, QH = 24576;
constexpr unsigned ZSS = 1638400, ZJ = 40960;
constexpr unsigned SPIN_LIMIT = 400000u;
constexpr int LDS_BYTES = 163840;

__device__ __forceinline__ int vblk() {
    const int b = blockIdx.x, x = b & 7, i = b >> 3;
    if (x == 0) return i;
    if (x < 4) { const int k = i * 3 + x - 1; return k < 80 ? 32 + k : k < 94 ? 180 + k - 80 : 160 + k; }
    const int r = i * 4 + x - 4;
    return r < 68 ? 112 + r : 126 + r;
}
template <int I> struct IC { static constexpr int v = I; };
template <int B, int E, class F> DEV void sfor(F&& f) { if constexpr (B < E) { f(IC<B>{}); sfor<B + 1, E>(f); } }

DEV unsigned pk(float a, float b) { v2f f = {a, b}; v2bf h = __builtin_convertvector(f, v2bf); return __builtin_bit_cast(unsigned, h); }
DEV float rbf(float a) { return (float)(__bf16)a; }
DEV float blo(unsigned u) { return __uint_as_float(u << 16); }
DEV float bhi(unsigned u) { return __uint_as_float(u & 0xffff0000u); }
DEV unsigned canon(unsigned u) { return u < 0xFFFFFFFEu ? u : 0xFFFFFFFEu; }
DEV v4f mfma(v4u a, v4u b, v4f c) { return __builtin_amdgcn_mfma_f32_16x16x32_bf16(__builtin_bit_cast(v8bf, a), __builtin_bit_cast(v8bf, b), c, 0, 0, 0); }
DEV v4f smf(v4u a, v8u b, v4f c, int idx) { return __builtin_amdgcn_smfmac_f32_16x16x64_bf16(__builtin_bit_cast(v8bf, a), __builtin_bit_cast(v16bf, b), c, idx, 0, 0); }
DEV v2u trd(unsigned a) { return __builtin_bit_cast(v2u, __builtin_amdgcn_ds_read_tr16_b64_v4i16((LDSP(v4s))(size_t)a)); }
DEV v4u lr128(unsigned a) { return *(LDSP(v4u))(size_t)a; }
DEV v4f lr128f(unsigned a) { return *(LDSP(v4f))(size_t)a; }
DEV void lw128f(unsigned a, v4f v) { *(LDSP(v4f))(size_t)a = v; }
DEV void lw128(unsigned a, v4u v) { *(LDSP(v4u))(size_t)a = v; }
DEV void lw64(unsigned a, v2u v) { *(LDSP(v2u))(size_t)a = v; }
DEV v4u lo4(v8u v) { return __builtin_shufflevector(v, v, 0, 1, 2, 3); }
DEV v4u hi4(v8u v) { return __builtin_shufflevector(v, v, 4, 5, 6, 7); }
DEV void bar() { asm volatile("s_waitcnt lgkmcnt(0)\n\ts_barrier" ::: "memory"); }
template <int N> DEV void waitvm() { asm volatile("s_waitcnt vmcnt(%0)" ::"i"(N) : "memory"); }
DEV void fence_c() { asm volatile("" ::: "memory"); }
DEV void glds(const void* g, unsigned dst) {
    unsigned keep;
    asm volatile("s_mov_b32 %0, m0\n\ts_mov_b32 m0, %2\n\ts_nop 0\n\tglobal_load_lds_dwordx4 %1, off nt\n\ts_mov_b32 m0, %0" : "=&s"(keep) : "v"(g), "s"(dst) : "memory");
}
DEV void glds_sc1(const void* g, unsigned dst) {
    unsigned keep;
    asm volatile("s_mov_b32 %0, m0\n\ts_mov_b32 m0, %2\n\ts_nop 0\n\tglobal_load_lds_dwordx4 %1, off sc1\n\ts_mov_b32 m0, %0" : "=&s"(keep) : "v"(g), "s"(dst) : "memory");
}
template <int N, bool SC1> DEV void gldsn(const char* base, unsigned vo, unsigned dst) {
    unsigned keep, vt;
    if constexpr (SC1)
        asm volatile("s_mov_b32 %0, m0\n\ts_mov_b32 m0, %4\n\tv_mov_b32 %1, %3\n\t.rept %c5\n\tglobal_load_lds_dwordx4 %1, %2 sc1\n\ts_add_u32 m0, m0, 0x400\n\tv_add_u32 %1, 0x400, %1\n\t.endr\n\ts_mov_b32 m0, %0"
                     : "=&s"(keep), "=&v"(vt) : "s"(base), "v"(vo), "s"(dst), "i"(N) : "memory", "scc");
    else
        asm volatile("s_mov_b32 %0, m0\n\ts_mov_b32 m0, %4\n\tv_mov_b32 %1, %3\n\t.rept %c5\n\tglobal_load_lds_dwordx4 %1, %2\n\ts_add_u32 m0, m0, 0x400\n\tv_add_u32 %1, 0x400, %1\n\t.endr\n\ts_mov_b32 m0, %0"
                     : "=&s"(keep), "=&v"(vt) : "s"(base), "v"(vo), "s"(dst), "i"(N) : "memory", "scc");
}
DEV rsrc_t mkr(const void* p, unsigned bytes) { return __builtin_amdgcn_make_buffer_rsrc((void*)p, 0, bytes, 0x00020000); }
DEV v4u ld128(rsrc_t r, unsigned vo, unsigned so) { return __builtin_amdgcn_raw_buffer_load_b128(r, vo, so, 16); }
DEV v2u ld64(rsrc_t r, unsigned vo, unsigned so) { return __builtin_amdgcn_raw_buffer_load_b64(r, vo, so, 16); }
DEV unsigned ld32(rsrc_t r, unsigned vo, unsigned so) { return __builtin_amdgcn_raw_buffer_load_b32(r, vo, so, 16); }
DEV void st128(rsrc_t r, unsigned vo, unsigned so, v4u v) { __builtin_amdgcn_raw_buffer_store_b128(v, r, vo, so, 16); asm volatile("s_nop 1" ::"v"(v) : "memory"); }
DEV void st64(rsrc_t r, unsigned vo, unsigned so, v2u v) { __builtin_amdgcn_raw_buffer_store_b64(v, r, vo, so, 16); }
DEV void st32(rsrc_t r, unsigned vo, unsigned so, unsigned v) { __builtin_amdgcn_raw_buffer_store_b32(v, r, vo, so, 16); }
DEV unsigned umax(unsigned a, unsigned b) { return a > b ? a : b; }
DEV unsigned mx4(v4u v) { return umax(umax(v.x, v.y), umax(v.z, v.w)); }
DEV unsigned mmap(unsigned p) { const unsigned c = p >> 10; return (c < 8 ? (c >> 1) * 16384 + (c & 1) * 1024 : (c - 8) * 16384 + 2048) + (p & 1023u); }
DEV unsigned mcu(unsigned j) { return mmap((j >> 1) * 256 + (j & 1) * 32); }
DEV unsigned mrow(unsigned r) { return (r >> 1) * 64 + (r & 1) * 16; }
DEV float xmax(float v) {
    auto a = __builtin_amdgcn_permlane16_swap(__float_as_uint(v), __float_as_uint(v), false, false);
    v = fmaxf(__uint_as_float(a[0]), __uint_as_float(a[1]));
    auto b = __builtin_amdgcn_permlane32_swap(__float_as_uint(v), __float_as_uint(v), false, false);
    return fmaxf(__uint_as_float(b[0]), __uint_as_float(b[1]));
}
DEV float xsum(float v) {
    auto a = __builtin_amdgcn_permlane16_swap(__float_as_uint(v), __float_as_uint(v), false, false);
    v = __uint_as_float(a[0]) + __uint_as_float(a[1]);
    auto b = __builtin_amdgcn_permlane32_swap(__float_as_uint(v), __float_as_uint(v), false, false);
    return __uint_as_float(b[0]) + __uint_as_float(b[1]);
}
DEV float hsum2(float a, float b) {
    auto r = __builtin_amdgcn_permlane32_swap(__float_as_uint(a), __float_as_uint(b), false, false);
    return __uint_as_float(r[0]) + __uint_as_float(r[1]);
}
DEV v2u hsum_pk(v4f a, v4f b) {
    v2u r; r.x = pk(hsum2(a[0], b[0]), hsum2(a[1], b[1])); r.y = pk(hsum2(a[2], b[2]), hsum2(a[3], b[3])); return r;
}

struct Params {
    const char* x; const char* mem0; const char* Wq; const char* Wk; const char* Wv; const char* Wo;
    const char* Wg; const char* Wu; const char* Wd; char* out; char* ws; unsigned long long mask;
};

DEV void code_touch(int n) {
    const char* pc;
    asm volatile("s_getpc_b64 %0" : "=s"(pc));
    const int q = (blockIdx.x >> 3) * 256 + threadIdx.x;
    if (q < n) asm volatile("global_load_dword a255, %0, %1" ::"v"(q * 128), "s"(pc) : "memory", "a255");
}
__global__ void __launch_bounds__(256) prep_kernel(Params P) {
    const int u = blockIdx.x * 256 + threadIdx.x;
    if (P.mask >> 32 & 1) code_touch(1200);
    constexpr int NF = (int)((FLAG_END - OFF_FLAG) / 16);
    static_assert(NF + 640 + 12288 + 640 <= 80 * 256, "");
    const unsigned f0 = (unsigned)(u * 16 - (OFF_FLAGX - OFF_FLAG)) < 640u;
    if (u < NF) *(v4u*)(P.ws + OFF_FLAG + u * 16) = v4u{f0, f0, f0, f0};
    else if (u < NF + 640) {
        const int q = u - NF;
        v4u v = *(const v4u*)(P.mem0 + (q & 7) * 1280 + (q >> 3) * 16);
        v.x = canon(v.x); v.y = canon(v.y); v.z = canon(v.z); v.w = canon(v.w);
        const unsigned a = mcu(q >> 3) + mrow(q & 7);
        *(v4u*)(P.ws + OFF_HAND + a) = v; *(v4u*)(P.ws + OFF_HAND + a + 8192) = v;
    } else if (u < NF + 640 + 12288) {
        const unsigned a = (u - NF - 640) * 16;
        const bool m0 = a < 65536u && (a & 8191u) < (a < 32768u ? 3072u : 2048u);
        if (!m0 && (a & 0xDC00u) != 51200u) *(v4u*)(P.ws + OFF_HAND + a) = v4u{~0u, ~0u, ~0u, ~0u};
    } else if (u < NF + 640 + 12288 + 640) {
        const int q = u - (NF + 640 + 12288);
        const char* a = P.mem0 + (q & 7) * 1280 + (q >> 4) * 32;
        const v4u d = *(const v4u*)a, e = *(const v4u*)(a + 16);
        char* o = P.ws + OFF_XNF - 40960 + (q >> 4) * 512 + (q & 15) * 16;
        *(v4u*)o = v4u{d.x, e.x, d.y, e.y}; *(v4u*)(o + 256) = v4u{d.z, e.z, d.w, e.w};
    }
}

template <int ROWS, int SEGU, int NI>
DEV void stage(const char* p0, const char* p1, unsigned ldb, int row0, unsigned dst, int w, int lane) {
    constexpr int UPR = 2 * SEGU + 2;
#pragma unroll
    for (int i = 0; i < NI; ++i) {
        const int u = (i * 4 + w) * 64 + lane;
        int row = u / UPR; const int cu = u - row * UPR; row = row < ROWS ? row : ROWS - 1;
        const char* src = cu < SEGU ? p0 + cu * 16 : (cu < 2 * SEGU ? p1 + (cu - SEGU) * 16 : p0);
        glds(src + (size_t)(row0 + row) * ldb, dst + (i * 4 + w) * 1024);
    }
}
DEV void wait_flags(rsrc_t r, unsigned vo, bool act, unsigned& dead) {
    for (;;) {
        fence_c();
        unsigned v = ld32(r, vo, 0);
        if (__all(!act || v != 0u)) break;
        if (dead == 0) break;
        --dead;
        __builtin_amdgcn_s_sleep(2);
    }
}
DEV void wait_slow(rsrc_t r, unsigned vo, int w, int n, unsigned& dead) {
    if (w == 0)
        for (;;) {
            fence_c();
            unsigned v = ld32(r, vo, 0);
            if (__all(v != 0u) || dead == 0) break;
            --dead;
            for (int i = 0; i < n; ++i) __builtin_amdgcn_s_sleep(2);
        }
    bar();
}
DEV void wait_ln(const char* ws, int lane, unsigned& dead) { wait_flags(mkr(ws + OFF_INIT, 1024), 640 + lane * 4, true, dead); }
DEV void wait_u(const char* ws, unsigned ivo, unsigned m, unsigned& rdy, unsigned& dead) {
    const rsrc_t r = mkr(ws + OFF_INIT, 1024);
    while ((rdy & m) != m && dead) {
        fence_c();
        rdy = (unsigned)__ballot(ld32(r, ivo, 0) != 0u);
        --dead;
        if ((rdy & m) != m) __builtin_amdgcn_s_sleep(4);
    }
}
DEV unsigned xcc_side() { return (__builtin_amdgcn_s_getreg((20) | (0 << 6) | (3 << 11)) >> 2) & 1u; }
DEV unsigned near_bits(const char* ws, int lane, unsigned& dead) {
    const rsrc_t r = mkr(ws + OFF_VOTE, 256);
    unsigned v;
    for (;;) {
        fence_c();
        v = ld32(r, lane * 4, 0);
        if (__all(v != 0u) || dead == 0) break;
        --dead;
        __builtin_amdgcn_s_sleep(4);
    }
    const unsigned long long up = __ballot(v > 8u), dn = __ballot(v < 8u), o = 0xAAAAAAAAAAAAAAAAull;
    const unsigned nb = (__popcll(up & ~o) > __popcll(dn & ~o) ? 1u : 0u) | (__popcll(up & o) > __popcll(dn & o) ? 2u : 0u);
    return nb ^ (xcc_side() * 3u);
}
DEV int probe(const char* a) {
    unsigned gap[2];
#pragma unroll
    for (int o = 0; o < 2; ++o) {
        unsigned long long t0, t1; unsigned v0, v1;
        asm volatile("global_load_dword %2, %4, off sc1\n\tglobal_load_dword %3, %5, off sc1\n\ts_waitcnt vmcnt(1)\n\ts_memtime %0\n\ts_waitcnt vmcnt(0)\n\ts_memtime %1\n\ts_waitcnt lgkmcnt(0)"
                     : "=&s"(t0), "=&s"(t1), "=&v"(v0), "=&v"(v1) : "v"(a + o * 8320), "v"(a + o * 128 + (1 - o) * 8192) : "memory");
        gap[o] = (unsigned)t1 - (unsigned)t0;
    }
    return gap[1] > gap[0] ? 1 : gap[1] < gap[0] ? -1 : 0;
}
DEV void ln_load(const char* x, int r0, int lane, v4u (&xa)[10]) {
    const char* p = x + (size_t)(r0 + (lane & 7)) * 1280 + (lane >> 3) * 32;
#pragma unroll
    for (int i = 0; i < 5; ++i)
        asm volatile("global_load_dwordx4 %0, %2, off offset:%c3\n\tglobal_load_dwordx4 %1, %2, off offset:%c4" : "=&v"(xa[2 * i]), "=&v"(xa[2 * i + 1]) : "v"(p), "i"(i * 256), "i"(i * 256 + 16) : "memory");
}
template <int N> DEV void ln_finish(char* ws, int r0, int lane, v4u (&xa)[10]) {
    asm volatile("s_waitcnt vmcnt(%c10)" : "+v"(xa[0]), "+v"(xa[1]), "+v"(xa[2]), "+v"(xa[3]), "+v"(xa[4]), "+v"(xa[5]), "+v"(xa[6]), "+v"(xa[7]), "+v"(xa[8]), "+v"(xa[9]) : "i"(N) : "memory");
    const rsrc_t rx = mkr(ws + OFF_XNF, 64 * 40960);
    const unsigned vo = (r0 >> 4) * 20480 + (lane >> 3) * 512 + ((r0 & 15) + (lane & 7)) * 16;
    v2f f[40], s2 = {0, 0}, v2 = {0, 0};
#pragma unroll
    for (int k = 0; k < 40; ++k) { f[k] = v2f{blo(xa[k >> 2][k & 3]), bhi(xa[k >> 2][k & 3])}; s2 += f[k]; }
    float s = s2.x + s2.y;
    s += __shfl_xor(s, 8);
    const float mean = xsum(s) * (1.f / 640.f);
#pragma unroll
    for (int k = 0; k < 40; ++k) { const v2f d = f[k] - mean; v2 += d * d; }
    float v = v2.x + v2.y;
    v += __shfl_xor(v, 8);
    v = xsum(v) * (1.f / 640.f);
    const float mb = rbf(mean), den = rbf(sqrtf(rbf(rbf(v) + 1e-5f))), rc = 1.f / den;
#pragma unroll
    for (int i = 0; i < 5; ++i) {
        unsigned d[8];
#pragma unroll
        for (int k = 0; k < 8; ++k) {
            const v2f t = f[8 * i + k] - mb; const unsigned u = pk(t.x, t.y);
            const v2f a = {blo(u), bhi(u)}; v2f q = a * rc;
            q += __builtin_elementwise_fma(-q, v2f{den, den}, a) * rc;
            d[k] = pk(q.x, q.y);
        }
        st128(rx, vo, i * 4096, v4u{d[0], d[4], d[1], d[5]});
        st128(rx, vo, i * 4096 + 256, v4u{d[2], d[6], d[3], d[7]});
    }
}
#define SB() __builtin_amdgcn_sched_barrier(0)
#define AC10(p) "a" p "0", "a" p "1", "a" p "2", "a" p "3", "a" p "4", "a" p "5", "a" p "6", "a" p "7", "a" p "8", "a" p "9"
#define ACLOB AC10(""), AC10("1"), AC10("2"), AC10("3"), AC10("4"), AC10("5"), AC10("6"), AC10("7"), AC10("8"), AC10("9"), AC10("10"), AC10("11"), AC10("12"), \
    AC10("13"), AC10("14"), AC10("15"), AC10("16"), AC10("17"), AC10("18"), AC10("19"), AC10("20"), AC10("21"), AC10("22"), AC10("23"), AC10("24"), "a250", "a251", "a252", "a253", "a254", "a255"
template <int R> DEV void aw4(unsigned x, unsigned y, unsigned z, unsigned w) {
    asm volatile("v_accvgpr_write_b32 a[%c4], %0\n\tv_accvgpr_write_b32 a[%c5], %1\n\tv_accvgpr_write_b32 a[%c6], %2\n\tv_accvgpr_write_b32 a[%c7], %3"
                 ::"v"(x), "v"(y), "v"(z), "v"(w), "i"(R), "i"(R + 1), "i"(R + 2), "i"(R + 3) : ACLOB);
}
template <int R> DEV void ald4(rsrc_t r, unsigned vo, unsigned so) {
    asm volatile("buffer_load_dwordx4 a[%c3:%c4], %0, %1, %2 offen sc1" ::"v"(vo), "s"(r), "s"(so), "i"(R), "i"(R + 3) : "memory", ACLOB);
}
DEV void vld4(v4u& d, rsrc_t r, unsigned vo, unsigned so) { asm volatile("buffer_load_dwordx4 %0, %1, %2, %3 offen sc1" : "=&v"(d) : "v"(vo), "s"(r), "s"(so) : "memory"); }
template <int R> DEV void smf_a(v4f& c, v4u a, int idx) { asm volatile("v_smfmac_f32_16x16x64_bf16 %0, %1, a[%c3:%c4], %2" : "+v"(c) : "v"(a), "v"(idx), "i"(R), "i"(R + 7) : ACLOB); }
DEV void smf_v(v4f& c, v4u a, v8u b, int idx) { asm volatile("v_smfmac_f32_16x16x64_bf16 %0, %1, %2, %3" : "+v"(c) : "v"(a), "v"(b), "v"(idx) : ACLOB); }
template <bool Z> DEV void mf_vv(v4f& c, v4u a, v4u b) {
    if constexpr (Z) asm volatile("v_mfma_f32_16x16x32_bf16 %0, %1, %2, 0" : "=&v"(c) : "v"(a), "v"(b) : ACLOB);
    else asm volatile("v_mfma_f32_16x16x32_bf16 %0, %1, %2, %0" : "+v"(c) : "v"(a), "v"(b) : ACLOB);
}
template <bool Z, int R> DEV void mf_va(v4f& c, v4u a) {
    if constexpr (Z) asm volatile("v_mfma_f32_16x16x32_bf16 %0, %1, a[%c2:%c3], 0" : "=&v"(c) : "v"(a), "i"(R), "i"(R + 3) : ACLOB);
    else asm volatile("v_mfma_f32_16x16x32_bf16 %0, %1, a[%c2:%c3], %0" : "+v"(c) : "v"(a), "i"(R), "i"(R + 3) : ACLOB);
}
template <bool Z, int R> DEV void mf_av(v4f& c, v4u b) {
    if constexpr (Z) asm volatile("v_mfma_f32_16x16x32_bf16 %0, a[%c2:%c3], %1, 0" : "=&v"(c) : "v"(b), "i"(R), "i"(R + 3) : ACLOB);
    else asm volatile("v_mfma_f32_16x16x32_bf16 %0, a[%c2:%c3], %1, %0" : "+v"(c) : "v"(b), "i"(R), "i"(R + 3) : ACLOB);
}
DEV void done2(v4f& a, v4f& b) { asm volatile("s_nop 7" : "+v"(a), "+v"(b)); }
DEV void done3(v4f& a, v4f& b, v4f& c) { asm volatile("s_nop 7" : "+v"(a), "+v"(b), "+v"(c)); }
DEV void done4(v4f& a, v4f& b, v4f& c, v4f& d) { asm volatile("s_nop 7" : "+v"(a), "+v"(b), "+v"(c), "+v"(d)); }
#define ZERO4 v4f{0, 0, 0, 0}

DEV void u_task(const Params& P, const int ut, const unsigned lb, const int w, const int lane) {
    const int i16 = lane & 15, g = lane >> 4, h = ut / 5, cb = ut - h * 5;
    char* ws = P.ws;
    const char* p0 = P.Wo + (size_t)h * 245760 + cb * 256;
    stage<192, 8, 14>(p0, p0 + 128, 1280, 0, lb, w, lane);
    unsigned avo[13];
#pragma unroll
    for (int i = 0; i < 13; ++i) { const int u = i * 64 + lane, r = u / 25, cu = u - r * 25; avo[i] = (r < 32 ? r : 31) * 12288 + (cu < 24 ? cu : 23) * 16; }
    const char* ap = P.Wv + h * 384 + (size_t)w * (160 * 12288);
    auto slot = [&](int k) { return lb + (k % 3 == 2 ? w * 14336 : 57344 + (w * 2 + k % 3) * 13312); };
    auto lda = [&](int k) {
        unsigned keep;
#define G1(n) "s_nop 0\n\tglobal_load_lds_dwordx4 %" #n ", %2 nt\n\ts_add_u32 m0, m0, 0x400\n\t"
        asm volatile("s_mov_b32 %0, m0\n\ts_mov_b32 m0, %1\n\t" G1(3) G1(4) G1(5) G1(6) G1(7) G1(8) G1(9) G1(10) G1(11) G1(12) G1(13) G1(14) G1(15) "s_mov_b32 m0, %0"
                     : "=&s"(keep) : "s"(slot(k)), "s"(ap + k * (32 * 12288)), "v"(avo[0]), "v"(avo[1]), "v"(avo[2]), "v"(avo[3]), "v"(avo[4]), "v"(avo[5]), "v"(avo[6]), "v"(avo[7]), "v"(avo[8]), "v"(avo[9]), "v"(avo[10]), "v"(avo[11]), "v"(avo[12]) : "memory", "scc");
    };
    lda(0); lda(1);
    waitvm<26>(); bar();
    {
        const unsigned tb = lb + (8 * g + (i16 >> 2)) * 288 + (i16 & 3) * 8;
        sfor<0, 48>([&](auto I) {
            constexpr int ot = I.v / 6, n = I.v % 6;
            const v2u r0 = trd(tb + n * 32 * 288 + ot * 32), r1 = trd(tb + (n * 32 + 4) * 288 + ot * 32);
            aw4<I.v * 4>(r0.x, r0.y, r1.x, r1.y);
        });
    }
    bar();
    lda(2);
    const unsigned ao = (16 * (i16 >> 3) + 4 * ((i16 >> 2) & 1) + 8 * ((i16 >> 1) & 1) + (i16 & 1)) * 400 + g * 16;
    v4u A[2][12];
    auto rda = [&](int k) {
#pragma unroll
        for (int q = 0; q < 12; ++q) A[k & 1][q] = lr128(slot(k) + ao + (q / 6) * 800 + (q % 6) * 64);
    };
    waitvm<26>(); rda(0);
    const rsrc_t ru = mkr(ws + OFF_UP, 80 * 327680);
    const unsigned uvo = (i16 >> 3) * 327680 + (g * 16 + (h & 1) * 8 + (i16 & 7)) * 16, uso = cb * (16 * 327680) + (h >> 1) * 20480 + w * 5120;
    v4f c[2][4];
    auto out = [&](int p) {
        v4f(&d)[4] = c[p & 1];
#pragma unroll
        for (int o = 0; o < 2; ++o)
            st128(ru, uvo, uso + ((p & 3) * 2 + o) * 655360 + (p >> 2) * 1024, v4u{pk(d[2 * o][0], d[2 * o][1]), pk(d[2 * o][2], d[2 * o][3]), pk(d[2 * o + 1][0], d[2 * o + 1][1]), pk(d[2 * o + 1][2], d[2 * o + 1][3])});
    };
    sfor<0, 20>([&](auto PP) {
        constexpr int p = PP.v, k = p >> 2, o0 = (p & 3) * 2;
        sfor<0, 6>([&](auto N) {
            constexpr int n = N.v;
            sfor<0, 4>([&](auto Q) { mf_va<n == 0, ((o0 + (Q.v >> 1)) * 6 + n) * 4>(c[p & 1][Q.v], A[k & 1][(Q.v & 1) * 6 + n]); });
            if constexpr (n == 0 && p > 0) { v4f(&d)[4] = c[(p - 1) & 1]; asm volatile("" : "+v"(d[0]), "+v"(d[1]), "+v"(d[2]), "+v"(d[3])); out(p - 1); }
            if constexpr (n == 0 && (p & 3) == 1 && k < 2) lda(k + 3);
            if constexpr (n == 0 && (p & 3) == 3 && k < 4) { waitvm<(k == 0 ? 32 : k == 1 ? 40 : k == 2 ? 33 : 20)>(); rda(k + 1); }
        });
    });
    done4(c[1][0], c[1][1], c[1][2], c[1][3]);
    out(19);
    waitvm<0>(); bar();
    if (w == 0 && lane == 0) st32(mkr(ws + OFF_INIT, 1024), ut * 4, 0, 1u);
}
DEV void ln_task(const Params& P, const int c, const int w, const int lane) {
    v4u xa[10];
    ln_load(P.x, (192 + c * 4 + w) * 8, lane, xa);
    ln_finish<0>(P.ws, (192 + c * 4 + w) * 8, lane, xa);
    const char* a = P.ws + OFF_HAND + ((w & 1) * 32 + c * 2 + (w >> 1)) * 65536 + 51200;
    (void)probe(a);
    bar();
    const rsrc_t ri = mkr(P.ws + OFF_INIT, 2048);
    if (w == 0 && lane == 0) st32(ri, (208 + c) * 4, 0, 1u);
    int v = 0;
#pragma unroll 1
    for (int i = 1; i < 4; ++i) v += probe(a + i * 256);
    if (lane == 0) st32(ri, 1024 + (c * 4 + w) * 4, 0, 8 + (xcc_side() ? -v : v));
}

#define VALUE_LOOP \
    "s_mov_b32 %[km0], m0\n\t" \
    "s_mov_b32 %[s], 0\n\t" \
    "s_mov_b32 %[s4], 0\n\t" \
    "s_mov_b32 %[sz], 0x80000000\n\t" \
    "s_mov_b32 %[s1], 0x80000000\n\t" \
    "s_mov_b32 %[sx], 0x80000000\n\t" \
    "v_add_u32 %[va], %[lb], %[l16]\n\t" \
    "s_waitcnt lgkmcnt(0)\n\t" \
    "ds_read_b128 v[44:47], %[va] offset:0\n\t" \
    "ds_read_b128 v[48:51], %[va] offset:1024\n\t" \
    "ds_read_b128 v[52:55], %[va] offset:2048\n\t" \
    "ds_read_b128 v[56:59], %[va] offset:3072\n\t" \
    "s_waitcnt lgkmcnt(3)\n\t" \
    "v_mfma_f32_16x16x32_bf16 v[60:63], v[44:47], a[0:3], 0\n\t" \
    "v_mfma_f32_16x16x32_bf16 v[64:67], v[44:47], a[64:67], 0\n\t" \
    "v_mfma_f32_16x16x32_bf16 v[68:71], v[44:47], a[128:131], 0\n\t" \
    "ds_read_b128 v[44:47], %[va] offset:4096\n\t" \
    "s_waitcnt lgkmcnt(3)\n\t" \
    "v_mfma_f32_16x16x32_bf16 v[60:63], v[48:51], a[4:7], v[60:63]\n\t" \
    "v_mfma_f32_16x16x32_bf16 v[64:67], v[48:51], a[68:71], v[64:67]\n\t" \
    "v_mfma_f32_16x16x32_bf16 v[68:71], v[48:51], a[132:135], v[68:71]\n\t" \
    "ds_read_b128 v[48:51], %[va] offset:5120\n\t" \
    "s_waitcnt lgkmcnt(3)\n\t" \
    "v_mfma_f32_16x16x32_bf16 v[60:63], v[52:55], a[8:11], v[60:63]\n\t" \
    "v_mfma_f32_16x16x32_bf16 v[64:67], v[52:55], a[72:75], v[64:67]\n\t" \
    "v_mfma_f32_16x16x32_bf16 v[68:71], v[52:55], a[136:139], v[68:71]\n\t" \
    "ds_read_b128 v[52:55], %[va] offset:6144\n\t" \
    "s_waitcnt lgkmcnt(3)\n\t" \
    "v_mfma_f32_16x16x32_bf16 v[60:63], v[56:59], a[12:15], v[60:63]\n\t" \
    "v_mfma_f32_16x16x32_bf16 v[64:67], v[56:59], a[76:79], v[64:67]\n\t" \
    "v_mfma_f32_16x16x32_bf16 v[68:71], v[56:59], a[140:143], v[68:71]\n\t" \
    "ds_read_b128 v[56:59], %[va] offset:7168\n\t" \
    "s_waitcnt lgkmcnt(3)\n\t" \
    "v_mfma_f32_16x16x32_bf16 v[60:63], v[44:47], a[16:19], v[60:63]\n\t" \
    "v_mfma_f32_16x16x32_bf16 v[64:67], v[44:47], a[80:83], v[64:67]\n\t" \
    "v_mfma_f32_16x16x32_bf16 v[68:71], v[44:47], a[144:147], v[68:71]\n\t" \
    "ds_read_b128 v[44:47], %[va] offset:8192\n\t" \
    "s_waitcnt lgkmcnt(3)\n\t" \
    "v_mfma_f32_16x16x32_bf16 v[60:63], v[48:51], a[20:23], v[60:63]\n\t" \
    "v_mfma_f32_16x16x32_bf16 v[64:67], v[48:51], a[84:87], v[64:67]\n\t" \
    "v_mfma_f32_16x16x32_bf16 v[68:71], v[48:51], a[148:151], v[68:71]\n\t" \
    "ds_read_b128 v[48:51], %[va] offset:9216\n\t" \
    "s_waitcnt lgkmcnt(3)\n\t" \
    "v_mfma_f32_16x16x32_bf16 v[60:63], v[52:55], a[24:27], v[60:63]\n\t" \
    "v_mfma_f32_16x16x32_bf16 v[64:67], v[52:55], a[88:91], v[64:67]\n\t" \
    "v_mfma_f32_16x16x32_bf16 v[68:71], v[52:55], a[152:155], v[68:71]\n\t" \
    "ds_read_b128 v[52:55], %[va] offset:10240\n\t" \
    "s_waitcnt lgkmcnt(3)\n\t" \
    "v_mfma_f32_16x16x32_bf16 v[60:63], v[56:59], a[28:31], v[60:63]\n\t" \
    "v_mfma_f32_16x16x32_bf16 v[64:67], v[56:59], a[92:95], v[64:67]\n\t" \
    "v_mfma_f32_16x16x32_bf16 v[68:71], v[56:59], a[156:159], v[68:71]\n\t" \
    "ds_read_b128 v[56:59], %[va] offset:11264\n\t" \
    "s_waitcnt lgkmcnt(3)\n\t" \
    "v_mfma_f32_16x16x32_bf16 v[60:63], v[44:47], a[32:35], v[60:63]\n\t" \
    "v_mfma_f32_16x16x32_bf16 v[64:67], v[44:47], a[96:99], v[64:67]\n\t" \
    "v_mfma_f32_16x16x32_bf16 v[68:71], v[44:47], a[160:163], v[68:71]\n\t" \
    "ds_read_b128 v[44:47], %[va] offset:12288\n\t" \
    "s_waitcnt lgkmcnt(3)\n\t" \
    "v_mfma_f32_16x16x32_bf16 v[60:63], v[48:51], a[36:39], v[60:63]\n\t" \
    "v_mfma_f32_16x16x32_bf16 v[64:67], v[48:51], a[100:103], v[64:67]\n\t" \
    "v_mfma_f32_16x16x32_bf16 v[68:71], v[48:51], a[164:167], v[68:71]\n\t" \
    "ds_read_b128 v[48:51], %[va] offset:13312\n\t" \
    "s_waitcnt lgkmcnt(0)\n\t" \
    "10:\n\t" \
    "s_add_u32 %[s1], %[s], 2\n\t" \
    "s_mul_i32 %[s2], %[s1], 40960\n\t" \
    "s_add_u32 %[s4], %[s4], 40960\n\t" \
    "s_cmp_eq_u32 %[s4], 122880\n\t" \
    "s_cselect_b32 %[s4], 0, %[s4]\n\t" \
    "s_add_u32 %[s1], %[s4], 40960\n\t" \
    "s_cmp_eq_u32 %[s1], 122880\n\t" \
    "s_cselect_b32 %[s1], 0, %[s1]\n\t" \
    "s_add_u32 m0, %[s1], %[ldx]\n\t" \
    "v_add_u32 %[t1], %[s2], %[l16]\n\t" \
    "s_add_u32 %[s1], %[sz], 0x1000\n\t" \
    "s_add_u32 %[s3], %[sz], 0x2000\n\t" \
    "s_mul_i32 %[sx], %[s], 1638400\n\t" \
    "s_add_u32 %[sx], %[sx], %[zb]\n\t" \
    "s_add_u32 %[sx], %[sx], 0x1000\n\t" \
    "s_cmp_eq_u32 %[s], 32\n\t" \
    "s_cselect_b32 %[cpo], %[cp1], %[cpo]\n\t" \
    "s_lshl_b32 %[so], %[s], 16\n\t" \
    "s_add_u32 %[so], %[so], %[cpo]\n\t" \
    "v_mfma_f32_16x16x32_bf16 v[60:63], v[52:55], a[40:43], v[60:63]\n\t" \
    "v_mfma_f32_16x16x32_bf16 v[64:67], v[52:55], a[104:107], v[64:67]\n\t" \
    "v_mfma_f32_16x16x32_bf16 v[68:71], v[52:55], a[168:171], v[68:71]\n\t" \
    "ds_read_b128 v[52:55], %[va] offset:14336\n\t" \
    "v_mfma_f32_16x16x32_bf16 v[60:63], v[56:59], a[44:47], v[60:63]\n\t" \
    "v_mfma_f32_16x16x32_bf16 v[64:67], v[56:59], a[108:111], v[64:67]\n\t" \
    "buffer_store_dwordx4 v[80:83], %[zvo], %[rz], %[sz] offen offset:0 sc1\n\t" \
    "v_mfma_f32_16x16x32_bf16 v[68:71], v[56:59], a[172:175], v[68:71]\n\t" \
    "ds_read_b128 v[56:59], %[va] offset:15360\n\t" \
    "v_mfma_f32_16x16x32_bf16 v[60:63], v[44:47], a[48:51], v[60:63]\n\t" \
    "v_mfma_f32_16x16x32_bf16 v[64:67], v[44:47], a[112:115], v[64:67]\n\t" \
    "v_mfma_f32_16x16x32_bf16 v[68:71], v[44:47], a[176:179], v[68:71]\n\t" \
    "ds_read_b128 v[44:47], %[va] offset:16384\n\t" \
    "v_mfma_f32_16x16x32_bf16 v[60:63], v[48:51], a[52:55], v[60:63]\n\t" \
    "v_mfma_f32_16x16x32_bf16 v[64:67], v[48:51], a[116:119], v[64:67]\n\t" \
    "v_mfma_f32_16x16x32_bf16 v[68:71], v[48:51], a[180:183], v[68:71]\n\t" \
    "global_load_lds_dwordx4 %[t1], %[xnf] sc1\n\t" \
    "s_add_u32 m0, m0, 1024\n\t" \
    "v_add_u32 %[t1], 1024, %[t1]\n\t" \
    "ds_read_b128 v[48:51], %[va] offset:17408\n\t" \
    "s_waitcnt lgkmcnt(3)\n\t" \
    "v_mfma_f32_16x16x32_bf16 v[60:63], v[52:55], a[56:59], v[60:63]\n\t" \
    "v_mfma_f32_16x16x32_bf16 v[64:67], v[52:55], a[120:123], v[64:67]\n\t" \
    "v_mfma_f32_16x16x32_bf16 v[68:71], v[52:55], a[184:187], v[68:71]\n\t" \
    "ds_read_b128 v[52:55], %[va] offset:18432\n\t" \
    "s_waitcnt lgkmcnt(3)\n\t" \
    "v_mfma_f32_16x16x32_bf16 v[60:63], v[56:59], a[60:63], v[60:63]\n\t" \
    "v_mfma_f32_16x16x32_bf16 v[64:67], v[56:59], a[124:127], v[64:67]\n\t" \
    "v_mfma_f32_16x16x32_bf16 v[68:71], v[56:59], a[188:191], v[68:71]\n\t" \
    "global_load_lds_dwordx4 %[t1], %[xnf] sc1\n\t" \
    "s_add_u32 m0, m0, 1024\n\t" \
    "v_add_u32 %[t1], 1024, %[t1]\n\t" \
    "ds_read_b128 v[56:59], %[va] offset:19456\n\t" \
    "s_waitcnt lgkmcnt(3)\n\t" \
    "v_mfma_f32_16x16x32_bf16 v[60:63], v[44:47], v[192:195], v[60:63]\n\t" \
    "v_mfma_f32_16x16x32_bf16 v[64:67], v[44:47], v[208:211], v[64:67]\n\t" \
    "v_mfma_f32_16x16x32_bf16 v[68:71], v[44:47], v[224:227], v[68:71]\n\t" \
    "ds_read_b128 v[44:47], %[va] offset:20480\n\t" \
    "s_waitcnt lgkmcnt(3)\n\t" \
    "v_mfma_f32_16x16x32_bf16 v[60:63], v[48:51], v[196:199], v[60:63]\n\t" \
    "v_mfma_f32_16x16x32_bf16 v[64:67], v[48:51], v[212:215], v[64:67]\n\t" \
    "v_mfma_f32_16x16x32_bf16 v[68:71], v[48:51], v[228:231], v[68:71]\n\t" \
    "global_load_lds_dwordx4 %[t1], %[xnf] sc1\n\t" \
    "s_add_u32 m0, m0, 1024\n\t" \
    "v_add_u32 %[t1], 1024, %[t1]\n\t" \
    "ds_read_b128 v[48:51], %[va] offset:21504\n\t" \
    "s_waitcnt lgkmcnt(3)\n\t" \
    "v_mfma_f32_16x16x32_bf16 v[60:63], v[52:55], v[200:203], v[60:63]\n\t" \
    "v_mfma_f32_16x16x32_bf16 v[64:67], v[52:55], v[216:219], v[64:67]\n\t" \
    "v_mfma_f32_16x16x32_bf16 v[68:71], v[52:55], v[232:235], v[68:71]\n\t" \
    "ds_read_b128 v[52:55], %[va] offset:22528\n\t" \
    "s_waitcnt lgkmcnt(3)\n\t" \
    "v_mfma_f32_16x16x32_bf16 v[60:63], v[56:59], v[204:207], v[60:63]\n\t" \
    "v_mfma_f32_16x16x32_bf16 v[64:67], v[56:59], v[220:223], v[64:67]\n\t" \
    "v_mfma_f32_16x16x32_bf16 v[68:71], v[56:59], v[236:239], v[68:71]\n\t" \
    "global_load_lds_dwordx4 %[t1], %[xnf] sc1\n\t" \
    "s_add_u32 m0, m0, 1024\n\t" \
    "v_add_u32 %[t1], 1024, %[t1]\n\t" \
    "ds_read_b128 v[56:59], %[va] offset:23552\n\t" \
    "s_waitcnt lgkmcnt(3)\n\t" \
    "v_mfma_f32_16x16x32_bf16 v[152:155], v[44:47], a[0:3], 0\n\t" \
    "v_mfma_f32_16x16x32_bf16 v[156:159], v[44:47], a[64:67], 0\n\t" \
    "v_mfma_f32_16x16x32_bf16 v[160:163], v[44:47], a[128:131], 0\n\t" \
    "ds_read_b128 v[44:47], %[va] offset:24576\n\t" \
    "s_waitcnt lgkmcnt(3)\n\t" \
    "v_mfma_f32_16x16x32_bf16 v[152:155], v[48:51], a[4:7], v[152:155]\n\t" \
    "v_mov_b64 v[168:169], 0\n\t" \
    "v_mov_b64 v[170:171], 0\n\t" \
    "v_mfma_f32_16x16x32_bf16 v[156:159], v[48:51], a[68:71], v[156:159]\n\t" \
    "v_mov_b64 v[172:173], 0\n\t" \
    "v_mov_b64 v[174:175], 0\n\t" \
    "v_mfma_f32_16x16x32_bf16 v[160:163], v[48:51], a[132:135], v[160:163]\n\t" \
    "global_load_lds_dwordx4 %[t1], %[xnf] sc1\n\t" \
    "s_add_u32 m0, m0, 16384\n\t" \
    "v_add_u32 %[t1], 16384, %[t1]\n\t" \
    "ds_read_b128 v[48:51], %[va] offset:25600\n\t" \
    "s_waitcnt lgkmcnt(3)\n\t" \
    "v_mfma_f32_16x16x32_bf16 v[152:155], v[52:55], a[8:11], v[152:155]\n\t" \
    "v_mov_b64 v[176:177], 0\n\t" \
    "v_mov_b64 v[178:179], 0\n\t" \
    "v_mfma_f32_16x16x32_bf16 v[156:159], v[52:55], a[72:75], v[156:159]\n\t" \
    "v_mov_b64 v[180:181], 0\n\t" \
    "v_mov_b64 v[182:183], 0\n\t" \
    "buffer_load_dwordx4 v[92:95], %[zvo], %[rz], %[sx] offen offset:2048 sc1\n\t" \
    "s_add_u32 %[sx], %[s], 1\n\t" \
    "s_mul_i32 %[sx], %[sx], 320\n\t" \
    "s_add_u32 %[sx], %[sx], %[fx]\n\t" \
    "v_mfma_f32_16x16x32_bf16 v[160:163], v[52:55], a[136:139], v[160:163]\n\t" \
    "ds_read_b128 v[52:55], %[va] offset:26624\n\t" \
    "buffer_load_dword v72, off, %[rf], %[sx] sc1\n\t" \
    "buffer_load_dwordx4 v[96:99], %[mvo0], %[rs], %[so] offen offset:0 sc1\n\t" \
    "buffer_load_dwordx4 v[100:103], %[mvo0], %[rs], %[so] offen offset:1024 sc1\n\t" \
    "buffer_load_dwordx4 v[104:107], %[mvo1], %[rs], %[so] offen offset:2048 sc1\n\t" \
    "s_waitcnt lgkmcnt(3)\n\t" \
    "v_mfma_f32_16x16x32_bf16 v[152:155], v[56:59], a[12:15], v[152:155]\n\t" \
    "v_mfma_f32_16x16x32_bf16 v[156:159], v[56:59], a[76:79], v[156:159]\n\t" \
    "v_mfma_f32_16x16x32_bf16 v[160:163], v[56:59], a[140:143], v[160:163]\n\t" \
    "global_load_lds_dwordx4 %[t1], %[xnf] sc1\n\t" \
    "s_add_u32 m0, m0, 1024\n\t" \
    "v_add_u32 %[t1], 1024, %[t1]\n\t" \
    "ds_read_b128 v[56:59], %[va] offset:27648\n\t" \
    "s_waitcnt lgkmcnt(3)\n\t" \
    "v_mfma_f32_16x16x32_bf16 v[152:155], v[44:47], a[16:19], v[152:155]\n\t" \
    "v_mfma_f32_16x16x32_bf16 v[156:159], v[44:47], a[80:83], v[156:159]\n\t" \
    "v_mfma_f32_16x16x32_bf16 v[160:163], v[44:47], a[144:147], v[160:163]\n\t" \
    "ds_read_b128 v[44:47], %[va] offset:28672\n\t" \
    "s_waitcnt lgkmcnt(3)\n\t" \
    "v_mfma_f32_16x16x32_bf16 v[152:155], v[48:51], a[20:23], v[152:155]\n\t" \
    "v_mfma_f32_16x16x32_bf16 v[156:159], v[48:51], a[84:87], v[156:159]\n\t" \
    "v_mfma_f32_16x16x32_bf16 v[160:163], v[48:51], a[148:151], v[160:163]\n\t" \
    "global_load_lds_dwordx4 %[t1], %[xnf] sc1\n\t" \
    "s_add_u32 m0, m0, 1024\n\t" \
    "v_add_u32 %[t1], 1024, %[t1]\n\t" \
    "ds_read_b128 v[48:51], %[va] offset:29696\n\t" \
    "s_waitcnt lgkmcnt(3)\n\t" \
    "v_mfma_f32_16x16x32_bf16 v[152:155], v[52:55], a[24:27], v[152:155]\n\t" \
    "v_mfma_f32_16x16x32_bf16 v[156:159], v[52:55], a[88:91], v[156:159]\n\t" \
    "v_mfma_f32_16x16x32_bf16 v[160:163], v[52:55], a[152:155], v[160:163]\n\t" \
    "ds_read_b128 v[52:55], %[va] offset:30720\n\t" \
    "s_waitcnt lgkmcnt(3)\n\t" \
    "v_mfma_f32_16x16x32_bf16 v[152:155], v[56:59], a[28:31], v[152:155]\n\t" \
    "v_mfma_f32_16x16x32_bf16 v[156:159], v[56:59], a[92:95], v[156:159]\n\t" \
    "v_mfma_f32_16x16x32_bf16 v[160:163], v[56:59], a[156:159], v[160:163]\n\t" \
    "global_load_lds_dwordx4 %[t1], %[xnf] sc1\n\t" \
    "s_add_u32 m0, m0, 1024\n\t" \
    "v_add_u32 %[t1], 1024, %[t1]\n\t" \
    "ds_read_b128 v[56:59], %[va] offset:31744\n\t" \
    "s_waitcnt lgkmcnt(3)\n\t" \
    "v_mfma_f32_16x16x32_bf16 v[152:155], v[44:47], a[32:35], v[152:155]\n\t" \
    "v_mfma_f32_16x16x32_bf16 v[156:159], v[44:47], a[96:99], v[156:159]\n\t" \
    "v_mfma_f32_16x16x32_bf16 v[160:163], v[44:47], a[160:163], v[160:163]\n\t" \
    "ds_read_b128 v[44:47], %[va] offset:32768\n\t" \
    "s_waitcnt lgkmcnt(3)\n\t" \
    "v_mfma_f32_16x16x32_bf16 v[152:155], v[48:51], a[36:39], v[152:155]\n\t" \
    "v_mfma_f32_16x16x32_bf16 v[156:159], v[48:51], a[100:103], v[156:159]\n\t" \
    "v_mfma_f32_16x16x32_bf16 v[160:163], v[48:51], a[164:167], v[160:163]\n\t" \
    "global_load_lds_dwordx4 %[t1], %[xnf] sc1\n\t" \
    "s_add_u32 m0, m0, 1024\n\t" \
    "v_add_u32 %[t1], 1024, %[t1]\n\t" \
    "ds_read_b128 v[48:51], %[va] offset:33792\n\t" \
    "s_waitcnt lgkmcnt(3)\n\t" \
    "v_mfma_f32_16x16x32_bf16 v[152:155], v[52:55], a[40:43], v[152:155]\n\t" \
    "v_mfma_f32_16x16x32_bf16 v[156:159], v[52:55], a[104:107], v[156:159]\n\t" \
    "v_mfma_f32_16x16x32_bf16 v[160:163], v[52:55], a[168:171], v[160:163]\n\t" \
    "ds_read_b128 v[52:55], %[va] offset:34816\n\t" \
    "s_waitcnt lgkmcnt(3)\n\t" \
    "v_mfma_f32_16x16x32_bf16 v[152:155], v[56:59], a[44:47], v[152:155]\n\t" \
    "v_mfma_f32_16x16x32_bf16 v[156:159], v[56:59], a[108:111], v[156:159]\n\t" \
    "v_mfma_f32_16x16x32_bf16 v[160:163], v[56:59], a[172:175], v[160:163]\n\t" \
    "global_load_lds_dwordx4 %[t1], %[xnf] sc1\n\t" \
    "ds_read_b128 v[56:59], %[va] offset:35840\n\t" \
    "s_waitcnt lgkmcnt(3)\n\t" \
    "v_mfma_f32_16x16x32_bf16 v[152:155], v[44:47], a[48:51], v[152:155]\n\t" \
    "v_mfma_f32_16x16x32_bf16 v[156:159], v[44:47], a[112:115], v[156:159]\n\t" \
    "buffer_store_dwordx4 v[84:87], %[zvo], %[rz], %[sz] offen offset:2048 sc1\n\t" \
    "v_mfma_f32_16x16x32_bf16 v[160:163], v[44:47], a[176:179], v[160:163]\n\t" \
    "ds_read_b128 v[44:47], %[va] offset:36864\n\t" \
    "s_waitcnt lgkmcnt(3)\n\t" \
    "v_mfma_f32_16x16x32_bf16 v[152:155], v[48:51], a[52:55], v[152:155]\n\t" \
    "v_mfma_f32_16x16x32_bf16 v[156:159], v[48:51], a[116:119], v[156:159]\n\t" \
    "buffer_store_dwordx4 v[88:91], %[zvo], %[rz], %[s1] offen offset:0 sc1\n\t" \
    "v_mfma_f32_16x16x32_bf16 v[160:163], v[48:51], a[180:183], v[160:163]\n\t" \
    "ds_read_b128 v[48:51], %[va] offset:37888\n\t" \
    "s_waitcnt lgkmcnt(3)\n\t" \
    "v_mfma_f32_16x16x32_bf16 v[152:155], v[52:55], a[56:59], v[152:155]\n\t" \
    "v_mfma_f32_16x16x32_bf16 v[156:159], v[52:55], a[120:123], v[156:159]\n\t" \
    "buffer_store_dwordx4 v[76:79], %[zvo], %[rz], %[s3] offen sc1\n\t" \
    "v_mfma_f32_16x16x32_bf16 v[160:163], v[52:55], a[184:187], v[160:163]\n\t" \
    "ds_read_b128 v[52:55], %[va] offset:38912\n\t" \
    "s_waitcnt lgkmcnt(3)\n\t" \
    "v_mfma_f32_16x16x32_bf16 v[152:155], v[56:59], a[60:63], v[152:155]\n\t" \
    "v_mfma_f32_16x16x32_bf16 v[156:159], v[56:59], a[124:127], v[156:159]\n\t" \
    "v_mfma_f32_16x16x32_bf16 v[160:163], v[56:59], a[188:191], v[160:163]\n\t" \
    "ds_read_b128 v[56:59], %[va] offset:39936\n\t" \
    "s_branch 14f\n\t" \
    "13:\n\t" \
    "buffer_load_dword v72, off, %[rf], %[sx] sc1\n\t" \
    "buffer_load_dwordx4 v[96:99], %[mvo0], %[rs], %[so] offen offset:0 sc1\n\t" \
    "buffer_load_dwordx4 v[100:103], %[mvo0], %[rs], %[so] offen offset:1024 sc1\n\t" \
    "buffer_load_dwordx4 v[104:107], %[mvo1], %[rs], %[so] offen offset:2048 sc1\n\t" \
    "s_waitcnt vmcnt(0)\n\t" \
    "14:\n\t" \
    "s_waitcnt vmcnt(10)\n\t" \
    "v_max3_u32 %[t0], v96, v97, v98\n\t" \
    "v_max_u32 %[t0], %[t0], v99\n\t" \
    "v_add_u32 v72, -1, v72\n\t" \
    "v_max_u32 %[t0], %[t0], v72\n\t" \
    "s_waitcnt vmcnt(9)\n\t" \
    "v_max3_u32 %[t0], %[t0], v100, v101\n\t" \
    "v_max3_u32 %[t0], %[t0], v102, v103\n\t" \
    "s_waitcnt vmcnt(8)\n\t" \
    "v_max3_u32 %[t0], %[t0], v104, v105\n\t" \
    "v_max3_u32 %[t0], %[t0], v106, v107\n\t" \
    "v_cmp_eq_u32 vcc, -1, %[t0]\n\t" \
    "s_cbranch_vccz 15f\n\t" \
    "s_cmp_eq_u32 %[cnt], 0\n\t" \
    "s_cbranch_scc1 15f\n\t" \
    "s_sub_u32 %[cnt], %[cnt], 1\n\t" \
    "s_branch 13b\n\t" \
    "15:\n\t" \
    "ds_write_b128 %[mw0], v[96:99] offset:0\n\t" \
    "ds_write_b128 %[mw0], v[100:103] offset:1024\n\t" \
    "ds_write_b128 %[mw1], v[104:107] offset:0\n\t" \
    "s_waitcnt lgkmcnt(6)\n\t" \
    "v_mfma_f32_16x16x32_bf16 v[152:155], v[44:47], v[192:195], v[152:155]\n\t" \
    "v_mfma_f32_16x16x32_bf16 v[156:159], v[44:47], v[208:211], v[156:159]\n\t" \
    "v_mfma_f32_16x16x32_bf16 v[160:163], v[44:47], v[224:227], v[160:163]\n\t" \
    "s_waitcnt lgkmcnt(0)\n\t" \
    "s_barrier\n\t" \
    "v_mfma_f32_16x16x32_bf16 v[152:155], v[48:51], v[196:199], v[152:155]\n\t" \
    "ds_read_b128 v[112:115], %[mr] offset:0\n\t" \
    "v_mfma_f32_16x16x32_bf16 v[156:159], v[48:51], v[212:215], v[156:159]\n\t" \
    "ds_read_b128 v[116:119], %[mr] offset:1024\n\t" \
    "v_mfma_f32_16x16x32_bf16 v[160:163], v[48:51], v[228:231], v[160:163]\n\t" \
    "ds_read_b128 v[120:123], %[mr] offset:2048\n\t" \
    "v_mfma_f32_16x16x32_bf16 v[152:155], v[52:55], v[200:203], v[152:155]\n\t" \
    "s_and_b32 %[s2], %[so], 0xffff3fff\n\t" \
    "buffer_load_dword v108, %[hvo], %[rs], %[s2] offen sc1\n\t" \
    "ds_read_b128 v[124:127], %[mr] offset:3072\n\t" \
    "v_mfma_f32_16x16x32_bf16 v[156:159], v[52:55], v[216:219], v[156:159]\n\t" \
    "ds_read_b128 v[128:131], %[mr] offset:4096\n\t" \
    "v_mfma_f32_16x16x32_bf16 v[160:163], v[52:55], v[232:235], v[160:163]\n\t" \
    "ds_read_b128 v[132:135], %[mr] offset:5120\n\t" \
    "v_mfma_f32_16x16x32_bf16 v[152:155], v[56:59], v[204:207], v[152:155]\n\t" \
    "ds_read_b128 v[136:139], %[mr] offset:6144\n\t" \
    "v_mfma_f32_16x16x32_bf16 v[156:159], v[56:59], v[220:223], v[156:159]\n\t" \
    "ds_read_b128 v[140:143], %[mr] offset:7168\n\t" \
    "v_mfma_f32_16x16x32_bf16 v[160:163], v[56:59], v[236:239], v[160:163]\n\t" \
    "ds_read_b128 v[144:147], %[mr] offset:8192\n\t" \
    "s_waitcnt lgkmcnt(8)\n\t" \
    "v_smfmac_f32_16x16x64_bf16 v[168:171], v[112:115], a[0:7], %[idx]\n\t" \
    "ds_read_b128 v[148:151], %[mr] offset:9216\n\t" \
    "v_smfmac_f32_16x16x64_bf16 v[172:175], v[112:115], a[64:71], %[idx]\n\t" \
    "v_smfmac_f32_16x16x64_bf16 v[176:179], v[112:115], a[128:135], %[idx]\n\t" \
    "v_smfmac_f32_16x16x64_bf16 v[180:183], v[112:115], a[192:199], %[idx]\n\t" \
    "s_waitcnt lgkmcnt(8)\n\t" \
    "v_smfmac_f32_16x16x64_bf16 v[168:171], v[116:119], a[8:15], %[idx]\n\t" \
    "s_add_u32 vcc_lo, %[s4], %[lb]\n\t" \
    "v_add_u32 %[va], vcc_lo, %[l16]\n\t" \
    "v_smfmac_f32_16x16x64_bf16 v[172:175], v[116:119], a[72:79], %[idx]\n\t" \
    "v_smfmac_f32_16x16x64_bf16 v[176:179], v[116:119], a[136:143], %[idx]\n\t" \
    "v_smfmac_f32_16x16x64_bf16 v[180:183], v[116:119], a[200:207], %[idx]\n\t" \
    "s_waitcnt lgkmcnt(7)\n\t" \
    "v_smfmac_f32_16x16x64_bf16 v[168:171], v[120:123], a[16:23], %[idx]\n\t" \
    "ds_read_b128 v[44:47], %[va] offset:0\n\t" \
    "v_smfmac_f32_16x16x64_bf16 v[172:175], v[120:123], a[80:87], %[idx]\n\t" \
    "ds_read_b128 v[48:51], %[va] offset:1024\n\t" \
    "v_smfmac_f32_16x16x64_bf16 v[176:179], v[120:123], a[144:151], %[idx]\n\t" \
    "ds_read_b128 v[52:55], %[va] offset:2048\n\t" \
    "v_smfmac_f32_16x16x64_bf16 v[180:183], v[120:123], a[208:215], %[idx]\n\t" \
    "ds_read_b128 v[56:59], %[va] offset:3072\n\t" \
    "s_waitcnt lgkmcnt(10)\n\t" \
    "v_smfmac_f32_16x16x64_bf16 v[168:171], v[124:127], a[24:31], %[idx]\n\t" \
    "v_smfmac_f32_16x16x64_bf16 v[172:175], v[124:127], a[88:95], %[idx]\n\t" \
    "v_smfmac_f32_16x16x64_bf16 v[176:179], v[124:127], a[152:159], %[idx]\n\t" \
    "v_smfmac_f32_16x16x64_bf16 v[180:183], v[124:127], a[216:223], %[idx]\n\t" \
    "s_waitcnt lgkmcnt(9)\n\t" \
    "v_smfmac_f32_16x16x64_bf16 v[168:171], v[128:131], a[32:39], %[idx]\n\t" \
    "v_smfmac_f32_16x16x64_bf16 v[172:175], v[128:131], a[96:103], %[idx]\n\t" \
    "v_smfmac_f32_16x16x64_bf16 v[176:179], v[128:131], a[160:167], %[idx]\n\t" \
    "v_smfmac_f32_16x16x64_bf16 v[180:183], v[128:131], a[224:231], %[idx]\n\t" \
    "s_waitcnt lgkmcnt(8)\n\t" \
    "v_smfmac_f32_16x16x64_bf16 v[168:171], v[132:135], a[40:47], %[idx]\n\t" \
    "v_smfmac_f32_16x16x64_bf16 v[172:175], v[132:135], a[104:111], %[idx]\n\t" \
    "v_smfmac_f32_16x16x64_bf16 v[176:179], v[132:135], a[168:175], %[idx]\n\t" \
    "v_smfmac_f32_16x16x64_bf16 v[180:183], v[132:135], a[232:239], %[idx]\n\t" \
    "s_waitcnt lgkmcnt(7)\n\t" \
    "v_smfmac_f32_16x16x64_bf16 v[168:171], v[136:139], a[48:55], %[idx]\n\t" \
    "v_smfmac_f32_16x16x64_bf16 v[172:175], v[136:139], a[112:119], %[idx]\n\t" \
    "v_smfmac_f32_16x16x64_bf16 v[176:179], v[136:139], a[176:183], %[idx]\n\t" \
    "v_smfmac_f32_16x16x64_bf16 v[180:183], v[136:139], a[240:247], %[idx]\n\t" \
    "s_waitcnt lgkmcnt(6)\n\t" \
    "v_smfmac_f32_16x16x64_bf16 v[168:171], v[140:143], a[56:63], %[idx]\n\t" \
    "v_cvt_pk_bf16_f32 v80, v60, v61\n\t" \
    "v_cvt_pk_bf16_f32 v81, v62, v63\n\t" \
    "v_smfmac_f32_16x16x64_bf16 v[172:175], v[140:143], a[120:127], %[idx]\n\t" \
    "v_cvt_pk_bf16_f32 v84, v64, v65\n\t" \
    "v_cvt_pk_bf16_f32 v85, v66, v67\n\t" \
    "v_smfmac_f32_16x16x64_bf16 v[176:179], v[140:143], a[184:191], %[idx]\n\t" \
    "v_cvt_pk_bf16_f32 v88, v68, v69\n\t" \
    "v_cvt_pk_bf16_f32 v89, v70, v71\n\t" \
    "v_smfmac_f32_16x16x64_bf16 v[180:183], v[140:143], a[248:255], %[idx]\n\t" \
    "s_waitcnt lgkmcnt(5)\n\t" \
    "v_smfmac_f32_16x16x64_bf16 v[168:171], v[144:147], v[192:199], %[idx]\n\t" \
    "s_mul_i32 %[sz], %[s], 1638400\n\t" \
    "s_add_u32 %[sz], %[sz], %[zb]\n\t" \
    "s_add_u32 %[s1], %[sz], 0x1000\n\t" \
    "s_add_u32 %[sx], %[sz], 0x2000\n\t" \
    "v_smfmac_f32_16x16x64_bf16 v[172:175], v[144:147], v[208:215], %[idx]\n\t" \
    "s_add_u32 %[s3], %[s], 1\n\t" \
    "s_lshl_b32 %[s3], %[s3], 16\n\t" \
    "s_add_u32 %[s3], %[s3], %[smo]\n\t" \
    "v_smfmac_f32_16x16x64_bf16 v[176:179], v[144:147], v[224:231], %[idx]\n\t" \
    "v_smfmac_f32_16x16x64_bf16 v[180:183], v[144:147], v[240:247], %[idx]\n\t" \
    "s_waitcnt lgkmcnt(4)\n\t" \
    "v_smfmac_f32_16x16x64_bf16 v[168:171], v[148:151], v[200:207], %[idx]\n\t" \
    "v_cvt_pk_bf16_f32 v82, v152, v153\n\t" \
    "v_cvt_pk_bf16_f32 v83, v154, v155\n\t" \
    "v_smfmac_f32_16x16x64_bf16 v[172:175], v[148:151], v[216:223], %[idx]\n\t" \
    "v_cvt_pk_bf16_f32 v86, v156, v157\n\t" \
    "v_cvt_pk_bf16_f32 v87, v158, v159\n\t" \
    "v_smfmac_f32_16x16x64_bf16 v[176:179], v[148:151], v[232:239], %[idx]\n\t" \
    "v_cvt_pk_bf16_f32 v90, v160, v161\n\t" \
    "v_cvt_pk_bf16_f32 v91, v162, v163\n\t" \
    "v_smfmac_f32_16x16x64_bf16 v[180:183], v[148:151], v[248:255], %[idx]\n\t" \
    "s_nop 7\n\t" \
    "v_permlane32_swap_b32 v168, v172\n\t" \
    "v_permlane32_swap_b32 v169, v173\n\t" \
    "v_permlane32_swap_b32 v170, v174\n\t" \
    "v_permlane32_swap_b32 v171, v175\n\t" \
    "v_pk_add_f32 v[168:169], v[168:169], v[172:173]\n\t" \
    "v_pk_add_f32 v[170:171], v[170:171], v[174:175]\n\t" \
    "v_cvt_pk_bf16_f32 v76, v168, v169\n\t" \
    "v_cvt_pk_bf16_f32 v77, v170, v171\n\t" \
    "v_permlane32_swap_b32 v176, v180\n\t" \
    "v_permlane32_swap_b32 v177, v181\n\t" \
    "v_permlane32_swap_b32 v178, v182\n\t" \
    "v_permlane32_swap_b32 v179, v183\n\t" \
    "v_pk_add_f32 v[176:177], v[176:177], v[180:181]\n\t" \
    "v_pk_add_f32 v[178:179], v[178:179], v[182:183]\n\t" \
    "v_cvt_pk_bf16_f32 v78, v176, v177\n\t" \
    "v_cvt_pk_bf16_f32 v79, v178, v179\n\t" \
    "s_cmp_lg_u32 %[s], 62\n\t" \
    "s_cbranch_scc1 20f\n\t" \
    "v_min_u32 v80, -2, v80\n\t" \
    "v_min_u32 v81, -2, v81\n\t" \
    "v_min_u32 v82, -2, v82\n\t" \
    "v_min_u32 v83, -2, v83\n\t" \
    "v_min_u32 v84, -2, v84\n\t" \
    "v_min_u32 v85, -2, v85\n\t" \
    "v_min_u32 v86, -2, v86\n\t" \
    "v_min_u32 v87, -2, v87\n\t" \
    "v_min_u32 v88, -2, v88\n\t" \
    "v_min_u32 v89, -2, v89\n\t" \
    "v_min_u32 v90, -2, v90\n\t" \
    "v_min_u32 v91, -2, v91\n\t" \
    "v_min_u32 v76, -2, v76\n\t" \
    "v_min_u32 v77, -2, v77\n\t" \
    "v_min_u32 v78, -2, v78\n\t" \
    "v_min_u32 v79, -2, v79\n\t" \
    "20:\n\t" \
    "s_waitcnt vmcnt(0)\n\t" \
    "s_branch 22f\n\t" \
    "21:\n\t" \
    "buffer_load_dword v108, %[hvo], %[rs], %[s2] offen sc1\n\t" \
    "s_waitcnt vmcnt(0)\n\t" \
    "22:\n\t" \
    "v_mov_b32 %[t0], v108\n\t" \
    "v_cmp_eq_u32 vcc, -1, %[t0]\n\t" \
    "s_cbranch_vccz 23f\n\t" \
    "s_cmp_eq_u32 %[cnt], 0\n\t" \
    "s_cbranch_scc1 23f\n\t" \
    "s_sub_u32 %[cnt], %[cnt], 1\n\t" \
    "s_branch 21b\n\t" \
    "23:\n\t" \
    "s_add_u32 %[s2], %[so], 3072\n\t" \
    "24:\n\t" \
    "s_memrealtime vcc\n\t" \
    "s_waitcnt lgkmcnt(0)\n\t" \
    "v_subrev_u32 %[t1], vcc_lo, %[t0]\n\t" \
    "v_cmp_gt_u32 vcc, 0x190, %[t1]\n\t" \
    "s_cbranch_vccnz 24b\n\t" \
    "s_cmp_eq_u32 %[s], 63\n\t" \
    "s_cbranch_scc1 12f\n\t" \
    "buffer_load_dwordx4 v[112:115], %[pvx], %[rs], %[s2] offen offset:0 sc1\n\t" \
    "buffer_load_dwordx4 v[116:119], %[pvx], %[rs], %[s2] offen offset:1280 sc1\n\t" \
    "buffer_load_dwordx4 v[120:123], %[pvx], %[rs], %[s2] offen offset:2560 sc1\n\t" \
    "buffer_load_dwordx4 v[124:127], %[pvx], %[rs], %[s2] offen offset:3840 sc1\n\t" \
    "buffer_load_dwordx2 v[128:129], %[pvm], %[rs], %[s2] offen offset:0 sc1\n\t" \
    "buffer_load_dwordx2 v[130:131], %[pvm], %[rs], %[s2] offen offset:2560 sc1\n\t" \
    "v_mfma_f32_16x16x32_bf16 v[60:63], v[44:47], a[0:3], 0\n\t" \
    "v_mfma_f32_16x16x32_bf16 v[64:67], v[44:47], a[64:67], 0\n\t" \
    "v_mfma_f32_16x16x32_bf16 v[68:71], v[44:47], a[128:131], 0\n\t" \
    "ds_read_b128 v[44:47], %[va] offset:4096\n\t" \
    "v_mfma_f32_16x16x32_bf16 v[60:63], v[48:51], a[4:7], v[60:63]\n\t" \
    "v_mfma_f32_16x16x32_bf16 v[64:67], v[48:51], a[68:71], v[64:67]\n\t" \
    "v_mfma_f32_16x16x32_bf16 v[68:71], v[48:51], a[132:135], v[68:71]\n\t" \
    "ds_read_b128 v[48:51], %[va] offset:5120\n\t" \
    "v_mfma_f32_16x16x32_bf16 v[60:63], v[52:55], a[8:11], v[60:63]\n\t" \
    "v_mfma_f32_16x16x32_bf16 v[64:67], v[52:55], a[72:75], v[64:67]\n\t" \
    "v_mfma_f32_16x16x32_bf16 v[68:71], v[52:55], a[136:139], v[68:71]\n\t" \
    "ds_read_b128 v[52:55], %[va] offset:6144\n\t" \
    "v_mfma_f32_16x16x32_bf16 v[60:63], v[56:59], a[12:15], v[60:63]\n\t" \
    "v_mfma_f32_16x16x32_bf16 v[64:67], v[56:59], a[76:79], v[64:67]\n\t" \
    "v_mfma_f32_16x16x32_bf16 v[68:71], v[56:59], a[140:143], v[68:71]\n\t" \
    "ds_read_b128 v[56:59], %[va] offset:7168\n\t" \
    "s_waitcnt lgkmcnt(3)\n\t" \
    "v_mfma_f32_16x16x32_bf16 v[60:63], v[44:47], a[16:19], v[60:63]\n\t" \
    "v_mfma_f32_16x16x32_bf16 v[64:67], v[44:47], a[80:83], v[64:67]\n\t" \
    "v_mfma_f32_16x16x32_bf16 v[68:71], v[44:47], a[144:147], v[68:71]\n\t" \
    "ds_read_b128 v[44:47], %[va] offset:8192\n\t" \
    "s_waitcnt lgkmcnt(3)\n\t" \
    "v_mfma_f32_16x16x32_bf16 v[60:63], v[48:51], a[20:23], v[60:63]\n\t" \
    "v_mfma_f32_16x16x32_bf16 v[64:67], v[48:51], a[84:87], v[64:67]\n\t" \
    "v_mfma_f32_16x16x32_bf16 v[68:71], v[48:51], a[148:151], v[68:71]\n\t" \
    "ds_read_b128 v[48:51], %[va] offset:9216\n\t" \
    "s_waitcnt lgkmcnt(3)\n\t" \
    "v_mfma_f32_16x16x32_bf16 v[60:63], v[52:55], a[24:27], v[60:63]\n\t" \
    "v_mfma_f32_16x16x32_bf16 v[64:67], v[52:55], a[88:91], v[64:67]\n\t" \
    "v_mfma_f32_16x16x32_bf16 v[68:71], v[52:55], a[152:155], v[68:71]\n\t" \
    "ds_read_b128 v[52:55], %[va] offset:10240\n\t" \
    "s_waitcnt lgkmcnt(3)\n\t" \
    "v_mfma_f32_16x16x32_bf16 v[60:63], v[56:59], a[28:31], v[60:63]\n\t" \
    "v_mfma_f32_16x16x32_bf16 v[64:67], v[56:59], a[92:95], v[64:67]\n\t" \
    "v_mfma_f32_16x16x32_bf16 v[68:71], v[56:59], a[156:159], v[68:71]\n\t" \
    "ds_read_b128 v[56:59], %[va] offset:11264\n\t" \
    "s_branch 16f\n\t" \
    "17:\n\t" \
    "buffer_load_dwordx4 v[112:115], %[pvx], %[rs], %[s2] offen offset:0 sc1\n\t" \
    "buffer_load_dwordx4 v[116:119], %[pvx], %[rs], %[s2] offen offset:1280 sc1\n\t" \
    "buffer_load_dwordx4 v[120:123], %[pvx], %[rs], %[s2] offen offset:2560 sc1\n\t" \
    "buffer_load_dwordx4 v[124:127], %[pvx], %[rs], %[s2] offen offset:3840 sc1\n\t" \
    "buffer_load_dwordx2 v[128:129], %[pvm], %[rs], %[s2] offen offset:0 sc1\n\t" \
    "buffer_load_dwordx2 v[130:131], %[pvm], %[rs], %[s2] offen offset:2560 sc1\n\t" \
    "s_waitcnt vmcnt(0)\n\t" \
    "16:\n\t" \
    "s_waitcnt vmcnt(5)\n\t" \
    "v_mfma_f32_16x16x32_bf16 v[184:187], v[80:83], v[112:115], 0\n\t" \
    "v_max3_u32 %[t0], v112, v113, v114\n\t" \
    "v_max_u32 %[t0], %[t0], v115\n\t" \
    "s_waitcnt vmcnt(4)\n\t" \
    "v_mfma_f32_16x16x32_bf16 v[184:187], v[84:87], v[116:119], v[184:187]\n\t" \
    "v_max3_u32 %[t0], %[t0], v116, v117\n\t" \
    "v_max3_u32 %[t0], %[t0], v118, v119\n\t" \
    "s_waitcnt vmcnt(3)\n\t" \
    "v_mfma_f32_16x16x32_bf16 v[184:187], v[88:91], v[120:123], v[184:187]\n\t" \
    "v_max3_u32 %[t0], %[t0], v120, v121\n\t" \
    "v_max3_u32 %[t0], %[t0], v122, v123\n\t" \
    "s_waitcnt vmcnt(2)\n\t" \
    "v_mfma_f32_16x16x32_bf16 v[184:187], v[92:95], v[124:127], v[184:187]\n\t" \
    "v_max3_u32 %[t0], %[t0], v124, v125\n\t" \
    "v_max3_u32 %[t0], %[t0], v126, v127\n\t" \
    "s_waitcnt vmcnt(0)\n\t" \
    "v_mfma_f32_16x16x32_bf16 v[184:187], v[76:79], v[128:131], v[184:187]\n\t" \
    "v_max3_u32 %[t0], %[t0], v128, v129\n\t" \
    "v_max3_u32 %[t0], %[t0], v130, v131\n\t" \
    "v_cmp_eq_u32 vcc, -1, %[t0]\n\t" \
    "s_cbranch_vccz 18f\n\t" \
    "s_cmp_eq_u32 %[cnt], 0\n\t" \
    "s_cbranch_scc1 18f\n\t" \
    "s_sub_u32 %[cnt], %[cnt], 1\n\t" \
    "s_branch 17b\n\t" \
    "18:\n\t" \
    "s_nop 3\n\t" \
    "ds_write_b128 %[redw], v[184:187]\n\t" \
    "s_waitcnt lgkmcnt(4)\n\t" \
    "v_mfma_f32_16x16x32_bf16 v[60:63], v[44:47], a[32:35], v[60:63]\n\t" \
    "v_mfma_f32_16x16x32_bf16 v[64:67], v[44:47], a[96:99], v[64:67]\n\t" \
    "v_mfma_f32_16x16x32_bf16 v[68:71], v[44:47], a[160:163], v[68:71]\n\t" \
    "s_waitcnt lgkmcnt(0)\n\t" \
    "s_barrier\n\t" \
    "ds_read2_b64 v[96:99], %[redr] offset1:32\n\t" \
    "ds_read2_b64 v[100:103], %[redr] offset0:64 offset1:96\n\t" \
    "ds_read_b128 v[44:47], %[va] offset:12288\n\t" \
    "v_mfma_f32_16x16x32_bf16 v[60:63], v[48:51], a[36:39], v[60:63]\n\t" \
    "v_mfma_f32_16x16x32_bf16 v[64:67], v[48:51], a[100:103], v[64:67]\n\t" \
    "v_mfma_f32_16x16x32_bf16 v[68:71], v[48:51], a[164:167], v[68:71]\n\t" \
    "ds_read_b128 v[48:51], %[va] offset:13312\n\t" \
    "s_cmp_lg_u32 %[w], 0\n\t" \
    "s_cbranch_scc1 19f\n\t" \
    "s_waitcnt lgkmcnt(2)\n\t" \
    "v_pk_add_f32 v[96:97], v[96:97], v[98:99]\n\t" \
    "v_pk_add_f32 v[100:101], v[100:101], v[102:103]\n\t" \
    "v_pk_add_f32 v[96:97], v[96:97], v[100:101]\n\t" \
    "v_mov_b64 v[98:99], v[96:97]\n\t" \
    "s_nop 1\n\t" \
    "v_permlane32_swap_b32 v96, v98\n\t" \
    "v_permlane32_swap_b32 v97, v99\n\t" \
    "v_pk_add_f32 v[96:97], v[96:97], v[98:99]\n\t" \
    "v_cvt_pk_bf16_f32 v96, v96, v97\n\t" \
    "v_min_u32 v96, -2, v96\n\t" \
    "s_nop 1\n\t" \
    "v_mov_b32_dpp v100, v96 quad_perm:[0,0,0,0] row_mask:0xf bank_mask:0xf\n\t" \
    "v_mov_b32_dpp v101, v96 quad_perm:[1,1,1,1] row_mask:0xf bank_mask:0xf\n\t" \
    "v_mov_b32_dpp v102, v96 quad_perm:[2,2,2,2] row_mask:0xf bank_mask:0xf\n\t" \
    "v_mov_b32_dpp v103, v96 quad_perm:[3,3,3,3] row_mask:0xf bank_mask:0xf\n\t" \
    "buffer_store_dwordx4 v[100:103], %[mso], %[rs], %[s3] offen sc1\n\t" \
    "19:\n\t" \
    "s_add_u32 %[s], %[s], 1\n\t" \
    "s_waitcnt lgkmcnt(0)\n\t" \
    "s_branch 10b\n\t" \
    "12:\n\t" \
    "s_mov_b32 m0, %[km0]\n\t"
#define SCORE_A \
    "s_waitcnt lgkmcnt(0)\n\t" \
    "v_add_u32 %[t1], 0x1a000, %[pa]\n\t" \
    "ds_read_b128 v[72:75], %[pa] offset:0\n\t" \
    "ds_read_b128 v[76:79], %[pa] offset:1024\n\t" \
    "ds_read_b128 v[80:83], %[pa] offset:8192\n\t" \
    "ds_read_b128 v[84:87], %[pa] offset:9216\n\t" \
    "v_mov_b64 v[48:49], 0\n\t" \
    "v_mov_b64 v[50:51], 0\n\t" \
    "v_mov_b64 v[52:53], 0\n\t" \
    "v_mov_b64 v[54:55], 0\n\t" \
    "v_mov_b64 v[56:57], 0\n\t" \
    "v_mov_b64 v[58:59], 0\n\t" \
    "v_mov_b64 v[60:61], 0\n\t" \
    "v_mov_b64 v[62:63], 0\n\t" \
    "v_mov_b64 v[96:97], 0\n\t" \
    "v_mov_b64 v[98:99], 0\n\t" \
    "v_mov_b64 v[100:101], 0\n\t" \
    "v_mov_b64 v[102:103], 0\n\t" \
    "s_waitcnt lgkmcnt(0)\n\t" \
    "s_cmp_lg_u32 %[w], 0\n\t" \
    "s_cbranch_scc1 30f\n\t" \
    "10:\n\t" \
    "buffer_load_dwordx4 v[64:67], %[mvo], %[rs], %[so] offen offset:0 sc1\n\t" \
    "buffer_load_dwordx4 v[68:71], %[mvo], %[rs], %[so] offen offset:1024 sc1\n\t" \
    "buffer_load_dwordx4 v[36:39], %[mvo], %[rs], %[so] offen offset:2048 sc1\n\t" \
    "s_waitcnt vmcnt(2)\n\t" \
    "v_max3_u32 %[t0], v64, v65, v66\n\t" \
    "v_max_u32 %[t0], %[t0], v67\n\t" \
    "s_waitcnt vmcnt(1)\n\t" \
    "v_max3_u32 %[t0], %[t0], v68, v69\n\t" \
    "v_max3_u32 %[t0], %[t0], v70, v71\n\t" \
    "s_waitcnt vmcnt(0)\n\t" \
    "v_max3_u32 %[t0], %[t0], v36, v37\n\t" \
    "v_max3_u32 %[t0], %[t0], v38, v39\n\t" \
    "v_cmp_eq_u32 vcc, -1, %[t0]\n\t" \
    "s_cbranch_vccz 20f\n\t" \
    "s_cmp_eq_u32 %[cnt], 0\n\t" \
    "s_cbranch_scc1 20f\n\t" \
    "s_sub_u32 %[cnt], %[cnt], 1\n\t" \
    "s_branch 10b\n\t" \
    "20:\n\t" \
    "s_memrealtime %[tz]\n\t" \
    "v_smfmac_f32_16x16x64_bf16 v[48:51], v[64:67], a[0:7], %[idx]\n\t" \
    "ds_write_b128 %[mw], v[36:39] offset:8192\n\t" \
    "v_smfmac_f32_16x16x64_bf16 v[52:55], v[64:67], a[8:15], %[idx]\n\t" \
    "ds_write_b128 %[mw], v[68:71] offset:1024\n\t" \
    "v_smfmac_f32_16x16x64_bf16 v[56:59], v[64:67], a[128:135], %[idx]\n\t" \
    "ds_write_b128 %[mw], v[64:67] offset:0\n\t" \
    "v_smfmac_f32_16x16x64_bf16 v[60:63], v[64:67], a[136:143], %[idx]\n\t" \
    "v_smfmac_f32_16x16x64_bf16 v[96:99], v[64:67], v[112:119], %[idx]\n\t" \
    "v_smfmac_f32_16x16x64_bf16 v[100:103], v[64:67], v[120:127], %[idx]\n\t" \
    "s_waitcnt lgkmcnt(0)\n\t" \
    "s_barrier\n\t" \
    "ds_read_b128 v[88:91], %[mw] offset:9216\n\t" \
    "ds_read_b128 v[92:95], %[mw] offset:2048\n\t" \
    "v_smfmac_f32_16x16x64_bf16 v[48:51], v[68:71], a[16:23], %[idx]\n\t" \
    "s_mov_b64 vcc, %[tz]\n\t" \
    "v_add_u32 %[t0], vcc_lo, %[dd]\n\t" \
    "ds_read_b128 v[28:31], %[mw] offset:3072\n\t" \
    "v_smfmac_f32_16x16x64_bf16 v[52:55], v[68:71], a[24:31], %[idx]\n\t" \
    "buffer_store_dword %[t0], %[hvo], %[rs], %[sh] offen sc1\n\t" \
    "ds_read_b128 v[32:35], %[mw] offset:4096\n\t" \
    "v_smfmac_f32_16x16x64_bf16 v[56:59], v[68:71], a[144:151], %[idx]\n\t" \
    "ds_read_b128 v[40:43], %[mw] offset:5120\n\t" \
    "v_smfmac_f32_16x16x64_bf16 v[60:63], v[68:71], a[152:159], %[idx]\n\t" \
    "ds_read_b128 v[44:47], %[mw] offset:6144\n\t" \
    "v_smfmac_f32_16x16x64_bf16 v[96:99], v[68:71], v[128:135], %[idx]\n\t" \
    "ds_read_b128 v[104:107], %[mw] offset:7168\n\t" \
    "v_smfmac_f32_16x16x64_bf16 v[100:103], v[68:71], v[136:143], %[idx]\n\t" \
    "s_waitcnt lgkmcnt(6)\n\t" \
    "v_smfmac_f32_16x16x64_bf16 v[48:51], v[88:91], v[72:79], %[idx]\n\t" \
    "ds_read_b128 v[72:75], %[t1] offset:0\n\t" \
    "ds_read_b128 v[76:79], %[t1] offset:1024\n\t" \
    "s_waitcnt lgkmcnt(7)\n\t" \
    "v_smfmac_f32_16x16x64_bf16 v[48:51], v[92:95], a[32:39], %[idx]\n\t" \
    "v_smfmac_f32_16x16x64_bf16 v[52:55], v[92:95], a[40:47], %[idx]\n\t" \
    "s_waitcnt lgkmcnt(6)\n\t" \
    "v_smfmac_f32_16x16x64_bf16 v[48:51], v[28:31], a[48:55], %[idx]\n\t" \
    "v_smfmac_f32_16x16x64_bf16 v[52:55], v[88:91], v[80:87], %[idx]\n\t" \
    "ds_read_b128 v[80:83], %[t1] offset:8192\n\t" \
    "ds_read_b128 v[84:87], %[t1] offset:9216\n\t" \
    "v_smfmac_f32_16x16x64_bf16 v[52:55], v[28:31], a[56:63], %[idx]\n\t" \
    "s_waitcnt lgkmcnt(7)\n\t" \
    "v_smfmac_f32_16x16x64_bf16 v[48:51], v[32:35], a[64:71], %[idx]\n\t" \
    "v_smfmac_f32_16x16x64_bf16 v[52:55], v[32:35], a[72:79], %[idx]\n\t" \
    "s_waitcnt lgkmcnt(2)\n\t" \
    "v_smfmac_f32_16x16x64_bf16 v[48:51], v[36:39], v[72:79], %[idx]\n\t" \
    "ds_read_b128 v[72:75], %[pa] offset:16384\n\t" \
    "ds_read_b128 v[76:79], %[pa] offset:17408\n\t" \
    "v_smfmac_f32_16x16x64_bf16 v[48:51], v[40:43], a[80:87], %[idx]\n\t" \
    "v_smfmac_f32_16x16x64_bf16 v[52:55], v[40:43], a[88:95], %[idx]\n\t" \
    "ds_read_b128 v[64:67], %[kb] offset:0\n\t" \
    "v_smfmac_f32_16x16x64_bf16 v[48:51], v[44:47], a[96:103], %[idx]\n\t" \
    "ds_read_b128 v[68:71], %[kb] offset:6144\n\t" \
    "s_waitcnt lgkmcnt(4)\n\t" \
    "v_smfmac_f32_16x16x64_bf16 v[52:55], v[36:39], v[80:87], %[idx]\n\t" \
    "ds_read_b128 v[80:83], %[pa] offset:24576\n\t" \
    "ds_read_b128 v[84:87], %[pa] offset:25600\n\t" \
    "v_smfmac_f32_16x16x64_bf16 v[52:55], v[44:47], a[104:111], %[idx]\n\t" \
    "v_smfmac_f32_16x16x64_bf16 v[48:51], v[104:107], a[112:119], %[idx]\n\t" \
    "v_smfmac_f32_16x16x64_bf16 v[52:55], v[104:107], a[120:127], %[idx]\n\t" \
    "s_waitcnt lgkmcnt(4)\n\t" \
    "v_smfmac_f32_16x16x64_bf16 v[56:59], v[88:91], v[72:79], %[idx]\n\t" \
    "ds_read_b128 v[72:75], %[t1] offset:16384\n\t" \
    "ds_read_b128 v[76:79], %[t1] offset:17408\n\t" \
    "v_smfmac_f32_16x16x64_bf16 v[56:59], v[92:95], a[160:167], %[idx]\n\t" \
    "v_smfmac_f32_16x16x64_bf16 v[60:63], v[92:95], a[168:175], %[idx]\n\t" \
    "v_smfmac_f32_16x16x64_bf16 v[56:59], v[28:31], a[176:183], %[idx]\n\t" \
    "s_waitcnt lgkmcnt(2)\n\t" \
    "v_smfmac_f32_16x16x64_bf16 v[60:63], v[88:91], v[80:87], %[idx]\n\t" \
    "ds_read_b128 v[80:83], %[t1] offset:24576\n\t" \
    "ds_read_b128 v[84:87], %[t1] offset:25600\n\t" \
    "v_smfmac_f32_16x16x64_bf16 v[60:63], v[28:31], a[184:191], %[idx]\n\t" \
    "v_smfmac_f32_16x16x64_bf16 v[56:59], v[32:35], a[192:199], %[idx]\n\t" \
    "v_smfmac_f32_16x16x64_bf16 v[60:63], v[32:35], a[200:207], %[idx]\n\t" \
    "v_pk_add_f32 v[48:49], v[48:49], v[50:51]\n\t" \
    "s_waitcnt lgkmcnt(2)\n\t" \
    "v_smfmac_f32_16x16x64_bf16 v[56:59], v[36:39], v[72:79], %[idx]\n\t" \
    "ds_read_b128 v[72:75], %[pa] offset:32768\n\t" \
    "ds_read_b128 v[76:79], %[pa] offset:33792\n\t" \
    "v_pk_add_f32 v[52:53], v[52:53], v[54:55]\n\t" \
    "v_smfmac_f32_16x16x64_bf16 v[56:59], v[40:43], a[208:215], %[idx]\n\t" \
    "v_cvt_pk_bf16_f32 v48, v48, v49\n\t" \
    "v_smfmac_f32_16x16x64_bf16 v[60:63], v[40:43], a[216:223], %[idx]\n\t" \
    "v_cvt_pk_bf16_f32 v49, v52, v53\n\t" \
    "v_smfmac_f32_16x16x64_bf16 v[56:59], v[44:47], a[224:231], %[idx]\n\t" \
    "ds_write_b64 %[xw], v[48:49] offset:0\n\t" \
    "s_waitcnt lgkmcnt(3)\n\t" \
    "v_smfmac_f32_16x16x64_bf16 v[60:63], v[36:39], v[80:87], %[idx]\n\t" \
    "ds_read_b128 v[80:83], %[pa] offset:40960\n\t" \
    "ds_read_b128 v[84:87], %[pa] offset:41984\n\t" \
    "ds_read_b128 v[48:51], %[kb] offset:1024\n\t" \
    "v_smfmac_f32_16x16x64_bf16 v[60:63], v[44:47], a[232:239], %[idx]\n\t" \
    "ds_read_b128 v[52:55], %[kb] offset:7168\n\t" \
    "v_smfmac_f32_16x16x64_bf16 v[56:59], v[104:107], a[240:247], %[idx]\n\t" \
    "v_smfmac_f32_16x16x64_bf16 v[60:63], v[104:107], a[248:255], %[idx]\n\t" \
    "s_waitcnt lgkmcnt(5)\n\t" \
    "v_smfmac_f32_16x16x64_bf16 v[96:99], v[88:91], v[72:79], %[idx]\n\t" \
    "ds_read_b128 v[72:75], %[kb] offset:2048\n\t" \
    "v_smfmac_f32_16x16x64_bf16 v[96:99], v[92:95], v[144:151], %[idx]\n\t" \
    "ds_read_b128 v[76:79], %[kb] offset:8192\n\t" \
    "v_smfmac_f32_16x16x64_bf16 v[100:103], v[92:95], v[152:159], %[idx]\n\t" \
    "ds_read_b128 v[92:95], %[kb] offset:10240\n\t" \
    "v_smfmac_f32_16x16x64_bf16 v[96:99], v[28:31], v[160:167], %[idx]\n\t" \
    "s_waitcnt lgkmcnt(5)\n\t" \
    "v_smfmac_f32_16x16x64_bf16 v[100:103], v[88:91], v[80:87], %[idx]\n\t" \
    "ds_read_b128 v[80:83], %[kb] offset:3072\n\t" \
    "v_smfmac_f32_16x16x64_bf16 v[100:103], v[28:31], v[168:175], %[idx]\n\t" \
    "ds_read_b128 v[84:87], %[kb] offset:9216\n\t" \
    "v_smfmac_f32_16x16x64_bf16 v[96:99], v[32:35], v[176:183], %[idx]\n\t" \
    "ds_read_b128 v[88:91], %[kb] offset:4096\n\t" \
    "v_smfmac_f32_16x16x64_bf16 v[100:103], v[32:35], v[184:191], %[idx]\n\t" \
    "v_pk_add_f32 v[56:57], v[56:57], v[58:59]\n\t" \
    "v_smfmac_f32_16x16x64_bf16 v[96:99], v[36:39], v[240:247], %[idx]\n\t" \
    "v_pk_add_f32 v[60:61], v[60:61], v[62:63]\n\t" \
    "v_smfmac_f32_16x16x64_bf16 v[100:103], v[36:39], v[248:255], %[idx]\n\t" \
    "v_cvt_pk_bf16_f32 v56, v56, v57\n\t" \
    "v_smfmac_f32_16x16x64_bf16 v[96:99], v[40:43], v[192:199], %[idx]\n\t" \
    "v_cvt_pk_bf16_f32 v57, v60, v61\n\t" \
    "v_smfmac_f32_16x16x64_bf16 v[100:103], v[40:43], v[200:207], %[idx]\n\t" \
    "ds_write_b64 %[xw], v[56:57] offset:512\n\t" \
    "v_smfmac_f32_16x16x64_bf16 v[96:99], v[44:47], v[208:215], %[idx]\n\t" \
    "ds_read_b128 v[56:59], %[kb] offset:5120\n\t" \
    "v_smfmac_f32_16x16x64_bf16 v[100:103], v[44:47], v[216:223], %[idx]\n\t" \
    "ds_read_b128 v[60:63], %[kb] offset:11264\n\t" \
    "v_smfmac_f32_16x16x64_bf16 v[96:99], v[104:107], v[224:231], %[idx]\n\t" \
    "v_smfmac_f32_16x16x64_bf16 v[100:103], v[104:107], v[232:239], %[idx]\n\t" \
    "s_nop 7\n\t" \
    "v_pk_add_f32 v[96:97], v[96:97], v[98:99]\n\t" \
    "v_pk_add_f32 v[100:101], v[100:101], v[102:103]\n\t" \
    "v_cvt_pk_bf16_f32 v96, v96, v97\n\t" \
    "v_cvt_pk_bf16_f32 v97, v100, v101\n\t" \
    "ds_write_b64 %[xw], v[96:97] offset:1024\n\t" \
    "s_branch 39f\n\t" \
    "30:\n\t" \
    "s_cmp_lg_u32 %[w], 1\n\t" \
    "s_cbranch_scc1 31f\n\t" \
    "11:\n\t" \
    "buffer_load_dwordx4 v[64:67], %[mvo], %[rs], %[so] offen offset:0 sc1\n\t" \
    "buffer_load_dwordx4 v[68:71], %[mvo], %[rs], %[so] offen offset:1024 sc1\n\t" \
    "buffer_load_dwordx4 v[88:91], %[mvo], %[rs], %[so] offen offset:2048 sc1\n\t" \
    "s_waitcnt vmcnt(2)\n\t" \
    "v_max3_u32 %[t0], v64, v65, v66\n\t" \
    "v_max_u32 %[t0], %[t0], v67\n\t" \
    "s_waitcnt vmcnt(1)\n\t" \
    "v_max3_u32 %[t0], %[t0], v68, v69\n\t" \
    "v_max3_u32 %[t0], %[t0], v70, v71\n\t" \
    "s_waitcnt vmcnt(0)\n\t" \
    "v_max3_u32 %[t0], %[t0], v88, v89\n\t" \
    "v_max3_u32 %[t0], %[t0], v90, v91\n\t" \
    "v_cmp_eq_u32 vcc, -1, %[t0]\n\t" \
    "s_cbranch_vccz 21f\n\t" \
    "s_cmp_eq_u32 %[cnt], 0\n\t" \
    "s_cbranch_scc1 21f\n\t" \
    "s_sub_u32 %[cnt], %[cnt], 1\n\t" \
    "s_branch 11b\n\t" \
    "21:\n\t" \
    "s_memrealtime %[tz]\n\t" \
    "v_smfmac_f32_16x16x64_bf16 v[48:51], v[64:67], a[32:39], %[idx]\n\t" \
    "ds_write_b128 %[mw], v[88:91] offset:9216\n\t" \
    "v_smfmac_f32_16x16x64_bf16 v[52:55], v[64:67], a[40:47], %[idx]\n\t" \
    "ds_write_b128 %[mw], v[68:71] offset:3072\n\t" \
    "v_smfmac_f32_16x16x64_bf16 v[56:59], v[64:67], a[160:167], %[idx]\n\t" \
    "ds_write_b128 %[mw], v[64:67] offset:2048\n\t" \
    "v_smfmac_f32_16x16x64_bf16 v[60:63], v[64:67], a[168:175], %[idx]\n\t" \
    "v_smfmac_f32_16x16x64_bf16 v[96:99], v[64:67], v[144:151], %[idx]\n\t" \
    "v_smfmac_f32_16x16x64_bf16 v[100:103], v[64:67], v[152:159], %[idx]\n\t" \
    "s_waitcnt lgkmcnt(0)\n\t" \
    "s_barrier\n\t" \
    "ds_read_b128 v[92:95], %[mw] offset:0\n\t" \
    "ds_read_b128 v[28:31], %[mw] offset:1024\n\t" \
    "v_smfmac_f32_16x16x64_bf16 v[48:51], v[68:71], a[48:55], %[idx]\n\t" \
    "s_mov_b64 vcc, %[tz]\n\t" \
    "v_add_u32 %[t0], vcc_lo, %[dd]\n\t" \
    "ds_read_b128 v[32:35], %[mw] offset:4096\n\t" \
    "v_smfmac_f32_16x16x64_bf16 v[52:55], v[68:71], a[56:63], %[idx]\n\t" \
    "buffer_store_dword %[t0], %[hvo], %[rs], %[sh] offen sc1\n\t" \
    "ds_read_b128 v[36:39], %[mw] offset:8192\n\t" \
    "v_smfmac_f32_16x16x64_bf16 v[56:59], v[68:71], a[176:183], %[idx]\n\t" \
    "ds_read_b128 v[40:43], %[mw] offset:5120\n\t" \
    "v_smfmac_f32_16x16x64_bf16 v[60:63], v[68:71], a[184:191], %[idx]\n\t" \
    "ds_read_b128 v[44:47], %[mw] offset:6144\n\t" \
    "v_smfmac_f32_16x16x64_bf16 v[96:99], v[68:71], v[160:167], %[idx]\n\t" \
    "ds_read_b128 v[104:107], %[mw] offset:7168\n\t" \
    "v_smfmac_f32_16x16x64_bf16 v[100:103], v[68:71], v[168:175], %[idx]\n\t" \
    "v_smfmac_f32_16x16x64_bf16 v[48:51], v[88:91], v[72:79], %[idx]\n\t" \
    "ds_read_b128 v[72:75], %[t1] offset:0\n\t" \
    "ds_read_b128 v[76:79], %[t1] offset:1024\n\t" \
    "s_waitcnt lgkmcnt(8)\n\t" \
    "v_smfmac_f32_16x16x64_bf16 v[48:51], v[92:95], a[0:7], %[idx]\n\t" \
    "v_smfmac_f32_16x16x64_bf16 v[52:55], v[92:95], a[8:15], %[idx]\n\t" \
    "s_waitcnt lgkmcnt(7)\n\t" \
    "v_smfmac_f32_16x16x64_bf16 v[48:51], v[28:31], a[16:23], %[idx]\n\t" \
    "v_smfmac_f32_16x16x64_bf16 v[52:55], v[88:91], v[80:87], %[idx]\n\t" \
    "ds_read_b128 v[80:83], %[t1] offset:8192\n\t" \
    "ds_read_b128 v[84:87], %[t1] offset:9216\n\t" \
    "v_smfmac_f32_16x16x64_bf16 v[52:55], v[28:31], a[24:31], %[idx]\n\t" \
    "s_waitcnt lgkmcnt(8)\n\t" \
    "v_smfmac_f32_16x16x64_bf16 v[48:51], v[32:35], a[64:71], %[idx]\n\t" \
    "v_smfmac_f32_16x16x64_bf16 v[52:55], v[32:35], a[72:79], %[idx]\n\t" \
    "s_waitcnt lgkmcnt(2)\n\t" \
    "v_smfmac_f32_16x16x64_bf16 v[48:51], v[36:39], v[72:79], %[idx]\n\t" \
    "ds_read_b128 v[72:75], %[pa] offset:16384\n\t" \
    "ds_read_b128 v[76:79], %[pa] offset:17408\n\t" \
    "v_smfmac_f32_16x16x64_bf16 v[48:51], v[40:43], a[80:87], %[idx]\n\t" \
    "v_smfmac_f32_16x16x64_bf16 v[52:55], v[40:43], a[88:95], %[idx]\n\t" \
    "ds_read_b128 v[64:67], %[kb] offset:0\n\t" \
    "v_smfmac_f32_16x16x64_bf16 v[48:51], v[44:47], a[96:103], %[idx]\n\t" \
    "ds_read_b128 v[68:71], %[kb] offset:6144\n\t" \
    "s_waitcnt lgkmcnt(4)\n\t" \
    "v_smfmac_f32_16x16x64_bf16 v[52:55], v[36:39], v[80:87], %[idx]\n\t" \
    "ds_read_b128 v[80:83], %[pa] offset:24576\n\t" \
    "ds_read_b128 v[84:87], %[pa] offset:25600\n\t" \
    "v_smfmac_f32_16x16x64_bf16 v[52:55], v[44:47], a[104:111], %[idx]\n\t" \
    "v_smfmac_f32_16x16x64_bf16 v[48:51], v[104:107], a[112:119], %[idx]\n\t" \
    "v_smfmac_f32_16x16x64_bf16 v[52:55], v[104:107], a[120:127], %[idx]\n\t" \
    "s_waitcnt lgkmcnt(4)\n\t" \
    "v_smfmac_f32_16x16x64_bf16 v[56:59], v[88:91], v[72:79], %[idx]\n\t" \
    "ds_read_b128 v[72:75], %[t1] offset:16384\n\t" \
    "ds_read_b128 v[76:79], %[t1] offset:17408\n\t" \
    "v_smfmac_f32_16x16x64_bf16 v[56:59], v[92:95], a[128:135], %[idx]\n\t" \
    "v_smfmac_f32_16x16x64_bf16 v[60:63], v[92:95], a[136:143], %[idx]\n\t" \
    "v_smfmac_f32_16x16x64_bf16 v[56:59], v[28:31], a[144:151], %[idx]\n\t" \
    "s_waitcnt lgkmcnt(2)\n\t" \
    "v_smfmac_f32_16x16x64_bf16 v[60:63], v[88:91], v[80:87], %[idx]\n\t" \
    "ds_read_b128 v[80:83], %[t1] offset:24576\n\t" \
    "ds_read_b128 v[84:87], %[t1] offset:25600\n\t" \
    "v_smfmac_f32_16x16x64_bf16 v[60:63], v[28:31], a[152:159], %[idx]\n\t" \
    "v_smfmac_f32_16x16x64_bf16 v[56:59], v[32:35], a[192:199], %[idx]\n\t" \
    "v_smfmac_f32_16x16x64_bf16 v[60:63], v[32:35], a[200:207], %[idx]\n\t" \
    "v_pk_add_f32 v[48:49], v[48:49], v[50:51]\n\t" \
    "s_waitcnt lgkmcnt(2)\n\t" \
    "v_smfmac_f32_16x16x64_bf16 v[56:59], v[36:39], v[72:79], %[idx]\n\t" \
    "ds_read_b128 v[72:75], %[pa] offset:32768\n\t" \
    "ds_read_b128 v[76:79], %[pa] offset:33792\n\t" \
    "v_pk_add_f32 v[52:53], v[52:53], v[54:55]\n\t" \
    "v_smfmac_f32_16x16x64_bf16 v[56:59], v[40:43], a[208:215], %[idx]\n\t" \
    "v_cvt_pk_bf16_f32 v48, v48, v49\n\t" \
    "v_smfmac_f32_16x16x64_bf16 v[60:63], v[40:43], a[216:223], %[idx]\n\t" \
    "v_cvt_pk_bf16_f32 v49, v52, v53\n\t" \
    "v_smfmac_f32_16x16x64_bf16 v[56:59], v[44:47], a[224:231], %[idx]\n\t" \
    "ds_write_b64 %[xw], v[48:49] offset:0\n\t" \
    "s_waitcnt lgkmcnt(3)\n\t" \
    "v_smfmac_f32_16x16x64_bf16 v[60:63], v[36:39], v[80:87], %[idx]\n\t" \
    "ds_read_b128 v[80:83], %[pa] offset:40960\n\t" \
    "ds_read_b128 v[84:87], %[pa] offset:41984\n\t" \
    "ds_read_b128 v[48:51], %[kb] offset:1024\n\t" \
    "v_smfmac_f32_16x16x64_bf16 v[60:63], v[44:47], a[232:239], %[idx]\n\t" \
    "ds_read_b128 v[52:55], %[kb] offset:7168\n\t" \
    "v_smfmac_f32_16x16x64_bf16 v[56:59], v[104:107], a[240:247], %[idx]\n\t" \
    "v_smfmac_f32_16x16x64_bf16 v[60:63], v[104:107], a[248:255], %[idx]\n\t" \
    "s_waitcnt lgkmcnt(5)\n\t" \
    "v_smfmac_f32_16x16x64_bf16 v[96:99], v[88:91], v[72:79], %[idx]\n\t" \
    "ds_read_b128 v[72:75], %[kb] offset:2048\n\t" \
    "v_smfmac_f32_16x16x64_bf16 v[96:99], v[92:95], v[112:119], %[idx]\n\t" \
    "ds_read_b128 v[76:79], %[kb] offset:8192\n\t" \
    "v_smfmac_f32_16x16x64_bf16 v[100:103], v[92:95], v[120:127], %[idx]\n\t" \
    "ds_read_b128 v[92:95], %[kb] offset:10240\n\t" \
    "v_smfmac_f32_16x16x64_bf16 v[96:99], v[28:31], v[128:135], %[idx]\n\t" \
    "s_waitcnt lgkmcnt(5)\n\t" \
    "v_smfmac_f32_16x16x64_bf16 v[100:103], v[88:91], v[80:87], %[idx]\n\t" \
    "ds_read_b128 v[80:83], %[kb] offset:3072\n\t" \
    "v_smfmac_f32_16x16x64_bf16 v[100:103], v[28:31], v[136:143], %[idx]\n\t" \
    "ds_read_b128 v[84:87], %[kb] offset:9216\n\t" \
    "v_smfmac_f32_16x16x64_bf16 v[96:99], v[32:35], v[176:183], %[idx]\n\t" \
    "ds_read_b128 v[88:91], %[kb] offset:4096\n\t" \
    "v_smfmac_f32_16x16x64_bf16 v[100:103], v[32:35], v[184:191], %[idx]\n\t" \
    "v_pk_add_f32 v[56:57], v[56:57], v[58:59]\n\t" \
    "v_smfmac_f32_16x16x64_bf16 v[96:99], v[36:39], v[240:247], %[idx]\n\t" \
    "v_pk_add_f32 v[60:61], v[60:61], v[62:63]\n\t" \
    "v_smfmac_f32_16x16x64_bf16 v[100:103], v[36:39], v[248:255], %[idx]\n\t" \
    "v_cvt_pk_bf16_f32 v56, v56, v57\n\t" \
    "v_smfmac_f32_16x16x64_bf16 v[96:99], v[40:43], v[192:199], %[idx]\n\t" \
    "v_cvt_pk_bf16_f32 v57, v60, v61\n\t" \
    "v_smfmac_f32_16x16x64_bf16 v[100:103], v[40:43], v[200:207], %[idx]\n\t" \
    "ds_write_b64 %[xw], v[56:57] offset:512\n\t" \
    "v_smfmac_f32_16x16x64_bf16 v[96:99], v[44:47], v[208:215], %[idx]\n\t" \
    "ds_read_b128 v[56:59], %[kb] offset:5120\n\t" \
    "v_smfmac_f32_16x16x64_bf16 v[100:103], v[44:47], v[216:223], %[idx]\n\t" \
    "ds_read_b128 v[60:63], %[kb] offset:11264\n\t" \
    "v_smfmac_f32_16x16x64_bf16 v[96:99], v[104:107], v[224:231], %[idx]\n\t" \
    "v_smfmac_f32_16x16x64_bf16 v[100:103], v[104:107], v[232:239], %[idx]\n\t" \
    "s_nop 7\n\t" \
    "v_pk_add_f32 v[96:97], v[96:97], v[98:99]\n\t" \
    "v_pk_add_f32 v[100:101], v[100:101], v[102:103]\n\t" \
    "v_cvt_pk_bf16_f32 v96, v96, v97\n\t" \
    "v_cvt_pk_bf16_f32 v97, v100, v101\n\t" \
    "ds_write_b64 %[xw], v[96:97] offset:1024\n\t" \
    "s_branch 39f\n\t" \
    "31:\n\t" \
    "s_cmp_lg_u32 %[w], 2\n\t" \
    "s_cbranch_scc1 32f\n\t" \
    "12:\n\t" \
    "buffer_load_dwordx4 v[64:67], %[mvo], %[rs], %[so] offen offset:0 sc1\n\t" \
    "buffer_load_dwordx4 v[68:71], %[mvo], %[rs], %[so] offen offset:1024 sc1\n\t" \
    "s_waitcnt vmcnt(1)\n\t" \
    "v_max3_u32 %[t0], v64, v65, v66\n\t" \
    "v_max_u32 %[t0], %[t0], v67\n\t" \
    "s_waitcnt vmcnt(0)\n\t" \
    "v_max3_u32 %[t0], %[t0], v68, v69\n\t" \
    "v_max3_u32 %[t0], %[t0], v70, v71\n\t" \
    "v_cmp_eq_u32 vcc, -1, %[t0]\n\t" \
    "s_cbranch_vccz 22f\n\t" \
    "s_cmp_eq_u32 %[cnt], 0\n\t" \
    "s_cbranch_scc1 22f\n\t" \
    "s_sub_u32 %[cnt], %[cnt], 1\n\t" \
    "s_branch 12b\n\t" \
    "22:\n\t" \
    "s_memrealtime %[tz]\n\t" \
    "v_smfmac_f32_16x16x64_bf16 v[48:51], v[64:67], a[64:71], %[idx]\n\t" \
    "ds_write_b128 %[mw], v[68:71] offset:5120\n\t" \
    "v_smfmac_f32_16x16x64_bf16 v[52:55], v[64:67], a[72:79], %[idx]\n\t" \
    "ds_write_b128 %[mw], v[64:67] offset:4096\n\t" \
    "v_smfmac_f32_16x16x64_bf16 v[56:59], v[64:67], a[192:199], %[idx]\n\t" \
    "v_smfmac_f32_16x16x64_bf16 v[60:63], v[64:67], a[200:207], %[idx]\n\t" \
    "v_smfmac_f32_16x16x64_bf16 v[96:99], v[64:67], v[176:183], %[idx]\n\t" \
    "v_smfmac_f32_16x16x64_bf16 v[100:103], v[64:67], v[184:191], %[idx]\n\t" \
    "s_waitcnt lgkmcnt(0)\n\t" \
    "s_barrier\n\t" \
    "ds_read_b128 v[88:91], %[mw] offset:9216\n\t" \
    "ds_read_b128 v[92:95], %[mw] offset:0\n\t" \
    "v_smfmac_f32_16x16x64_bf16 v[48:51], v[68:71], a[80:87], %[idx]\n\t" \
    "s_mov_b64 vcc, %[tz]\n\t" \
    "v_add_u32 %[t0], vcc_lo, %[dd]\n\t" \
    "ds_read_b128 v[28:31], %[mw] offset:1024\n\t" \
    "v_smfmac_f32_16x16x64_bf16 v[52:55], v[68:71], a[88:95], %[idx]\n\t" \
    "buffer_store_dword %[t0], %[hvo], %[rs], %[sh] offen sc1\n\t" \
    "ds_read_b128 v[32:35], %[mw] offset:2048\n\t" \
    "v_smfmac_f32_16x16x64_bf16 v[56:59], v[68:71], a[208:215], %[idx]\n\t" \
    "ds_read_b128 v[36:39], %[mw] offset:8192\n\t" \
    "v_smfmac_f32_16x16x64_bf16 v[60:63], v[68:71], a[216:223], %[idx]\n\t" \
    "ds_read_b128 v[40:43], %[mw] offset:3072\n\t" \
    "v_smfmac_f32_16x16x64_bf16 v[96:99], v[68:71], v[192:199], %[idx]\n\t" \
    "ds_read_b128 v[44:47], %[mw] offset:6144\n\t" \
    "v_smfmac_f32_16x16x64_bf16 v[100:103], v[68:71], v[200:207], %[idx]\n\t" \
    "ds_read_b128 v[104:107], %[mw] offset:7168\n\t" \
    "s_waitcnt lgkmcnt(7)\n\t" \
    "v_smfmac_f32_16x16x64_bf16 v[48:51], v[88:91], v[72:79], %[idx]\n\t" \
    "ds_read_b128 v[72:75], %[t1] offset:0\n\t" \
    "ds_read_b128 v[76:79], %[t1] offset:1024\n\t" \
    "s_waitcnt lgkmcnt(8)\n\t" \
    "v_smfmac_f32_16x16x64_bf16 v[48:51], v[92:95], a[0:7], %[idx]\n\t" \
    "v_smfmac_f32_16x16x64_bf16 v[52:55], v[92:95], a[8:15], %[idx]\n\t" \
    "s_waitcnt lgkmcnt(7)\n\t" \
    "v_smfmac_f32_16x16x64_bf16 v[48:51], v[28:31], a[16:23], %[idx]\n\t" \
    "v_smfmac_f32_16x16x64_bf16 v[52:55], v[88:91], v[80:87], %[idx]\n\t" \
    "ds_read_b128 v[80:83], %[t1] offset:8192\n\t" \
    "ds_read_b128 v[84:87], %[t1] offset:9216\n\t" \
    "v_smfmac_f32_16x16x64_bf16 v[52:55], v[28:31], a[24:31], %[idx]\n\t" \
    "s_waitcnt lgkmcnt(8)\n\t" \
    "v_smfmac_f32_16x16x64_bf16 v[48:51], v[32:35], a[32:39], %[idx]\n\t" \
    "v_smfmac_f32_16x16x64_bf16 v[52:55], v[32:35], a[40:47], %[idx]\n\t" \
    "s_waitcnt lgkmcnt(2)\n\t" \
    "v_smfmac_f32_16x16x64_bf16 v[48:51], v[36:39], v[72:79], %[idx]\n\t" \
    "ds_read_b128 v[72:75], %[pa] offset:16384\n\t" \
    "ds_read_b128 v[76:79], %[pa] offset:17408\n\t" \
    "v_smfmac_f32_16x16x64_bf16 v[48:51], v[40:43], a[48:55], %[idx]\n\t" \
    "v_smfmac_f32_16x16x64_bf16 v[52:55], v[40:43], a[56:63], %[idx]\n\t" \
    "ds_read_b128 v[64:67], %[kb] offset:0\n\t" \
    "v_smfmac_f32_16x16x64_bf16 v[48:51], v[44:47], a[96:103], %[idx]\n\t" \
    "ds_read_b128 v[68:71], %[kb] offset:6144\n\t" \
    "s_waitcnt lgkmcnt(4)\n\t" \
    "v_smfmac_f32_16x16x64_bf16 v[52:55], v[36:39], v[80:87], %[idx]\n\t" \
    "ds_read_b128 v[80:83], %[pa] offset:24576\n\t" \
    "ds_read_b128 v[84:87], %[pa] offset:25600\n\t" \
    "v_smfmac_f32_16x16x64_bf16 v[52:55], v[44:47], a[104:111], %[idx]\n\t" \
    "v_smfmac_f32_16x16x64_bf16 v[48:51], v[104:107], a[112:119], %[idx]\n\t" \
    "v_smfmac_f32_16x16x64_bf16 v[52:55], v[104:107], a[120:127], %[idx]\n\t" \
    "s_waitcnt lgkmcnt(4)\n\t" \
    "v_smfmac_f32_16x16x64_bf16 v[56:59], v[88:91], v[72:79], %[idx]\n\t" \
    "ds_read_b128 v[72:75], %[t1] offset:16384\n\t" \
    "ds_read_b128 v[76:79], %[t1] offset:17408\n\t" \
    "v_smfmac_f32_16x16x64_bf16 v[56:59], v[92:95], a[128:135], %[idx]\n\t" \
    "v_smfmac_f32_16x16x64_bf16 v[60:63], v[92:95], a[136:143], %[idx]\n\t" \
    "v_smfmac_f32_16x16x64_bf16 v[56:59], v[28:31], a[144:151], %[idx]\n\t" \
    "s_waitcnt lgkmcnt(2)\n\t" \
    "v_smfmac_f32_16x16x64_bf16 v[60:63], v[88:91], v[80:87], %[idx]\n\t" \
    "ds_read_b128 v[80:83], %[t1] offset:24576\n\t" \
    "ds_read_b128 v[84:87], %[t1] offset:25600\n\t" \
    "v_smfmac_f32_16x16x64_bf16 v[60:63], v[28:31], a[152:159], %[idx]\n\t" \
    "v_smfmac_f32_16x16x64_bf16 v[56:59], v[32:35], a[160:167], %[idx]\n\t" \
    "v_smfmac_f32_16x16x64_bf16 v[60:63], v[32:35], a[168:175], %[idx]\n\t" \
    "v_pk_add_f32 v[48:49], v[48:49], v[50:51]\n\t" \
    "s_waitcnt lgkmcnt(2)\n\t" \
    "v_smfmac_f32_16x16x64_bf16 v[56:59], v[36:39], v[72:79], %[idx]\n\t" \
    "ds_read_b128 v[72:75], %[pa] offset:32768\n\t" \
    "ds_read_b128 v[76:79], %[pa] offset:33792\n\t" \
    "v_pk_add_f32 v[52:53], v[52:53], v[54:55]\n\t" \
    "v_smfmac_f32_16x16x64_bf16 v[56:59], v[40:43], a[176:183], %[idx]\n\t" \
    "v_cvt_pk_bf16_f32 v48, v48, v49\n\t" \
    "v_smfmac_f32_16x16x64_bf16 v[60:63], v[40:43], a[184:191], %[idx]\n\t" \
    "v_cvt_pk_bf16_f32 v49, v52, v53\n\t" \
    "v_smfmac_f32_16x16x64_bf16 v[56:59], v[44:47], a[224:231], %[idx]\n\t" \
    "ds_write_b64 %[xw], v[48:49] offset:0\n\t" \
    "s_waitcnt lgkmcnt(3)\n\t" \
    "v_smfmac_f32_16x16x64_bf16 v[60:63], v[36:39], v[80:87], %[idx]\n\t" \
    "ds_read_b128 v[80:83], %[pa] offset:40960\n\t" \
    "ds_read_b128 v[84:87], %[pa] offset:41984\n\t" \
    "ds_read_b128 v[48:51], %[kb] offset:1024\n\t" \
    "v_smfmac_f32_16x16x64_bf16 v[60:63], v[44:47], a[232:239], %[idx]\n\t" \
    "ds_read_b128 v[52:55], %[kb] offset:7168\n\t" \
    "v_smfmac_f32_16x16x64_bf16 v[56:59], v[104:107], a[240:247], %[idx]\n\t" \
    "v_smfmac_f32_16x16x64_bf16 v[60:63], v[104:107], a[248:255], %[idx]\n\t" \
    "s_waitcnt lgkmcnt(5)\n\t" \
    "v_smfmac_f32_16x16x64_bf16 v[96:99], v[88:91], v[72:79], %[idx]\n\t" \
    "ds_read_b128 v[72:75], %[kb] offset:2048\n\t" \
    "v_smfmac_f32_16x16x64_bf16 v[96:99], v[92:95], v[112:119], %[idx]\n\t" \
    "ds_read_b128 v[76:79], %[kb] offset:8192\n\t" \
    "v_smfmac_f32_16x16x64_bf16 v[100:103], v[92:95], v[120:127], %[idx]\n\t" \
    "ds_read_b128 v[92:95], %[kb] offset:10240\n\t" \
    "v_smfmac_f32_16x16x64_bf16 v[96:99], v[28:31], v[128:135], %[idx]\n\t" \
    "s_waitcnt lgkmcnt(5)\n\t" \
    "v_smfmac_f32_16x16x64_bf16 v[100:103], v[88:91], v[80:87], %[idx]\n\t" \
    "ds_read_b128 v[80:83], %[kb] offset:3072\n\t" \
    "v_smfmac_f32_16x16x64_bf16 v[100:103], v[28:31], v[136:143], %[idx]\n\t" \
    "ds_read_b128 v[84:87], %[kb] offset:9216\n\t" \
    "v_smfmac_f32_16x16x64_bf16 v[96:99], v[32:35], v[144:151], %[idx]\n\t" \
    "ds_read_b128 v[88:91], %[kb] offset:4096\n\t" \
    "v_smfmac_f32_16x16x64_bf16 v[100:103], v[32:35], v[152:159], %[idx]\n\t" \
    "v_pk_add_f32 v[56:57], v[56:57], v[58:59]\n\t" \
    "v_smfmac_f32_16x16x64_bf16 v[96:99], v[36:39], v[240:247], %[idx]\n\t" \
    "v_pk_add_f32 v[60:61], v[60:61], v[62:63]\n\t" \
    "v_smfmac_f32_16x16x64_bf16 v[100:103], v[36:39], v[248:255], %[idx]\n\t" \
    "v_cvt_pk_bf16_f32 v56, v56, v57\n\t" \
    "v_smfmac_f32_16x16x64_bf16 v[96:99], v[40:43], v[160:167], %[idx]\n\t" \
    "v_cvt_pk_bf16_f32 v57, v60, v61\n\t" \
    "v_smfmac_f32_16x16x64_bf16 v[100:103], v[40:43], v[168:175], %[idx]\n\t" \
    "ds_write_b64 %[xw], v[56:57] offset:512\n\t" \
    "v_smfmac_f32_16x16x64_bf16 v[96:99], v[44:47], v[208:215], %[idx]\n\t" \
    "ds_read_b128 v[56:59], %[kb] offset:5120\n\t" \
    "v_smfmac_f32_16x16x64_bf16 v[100:103], v[44:47], v[216:223], %[idx]\n\t" \
    "ds_read_b128 v[60:63], %[kb] offset:11264\n\t" \
    "v_smfmac_f32_16x16x64_bf16 v[96:99], v[104:107], v[224:231], %[idx]\n\t" \
    "v_smfmac_f32_16x16x64_bf16 v[100:103], v[104:107], v[232:239], %[idx]\n\t" \
    "s_nop 7\n\t" \
    "v_pk_add_f32 v[96:97], v[96:97], v[98:99]\n\t" \
    "v_pk_add_f32 v[100:101], v[100:101], v[102:103]\n\t" \
    "v_cvt_pk_bf16_f32 v96, v96, v97\n\t" \
    "v_cvt_pk_bf16_f32 v97, v100, v101\n\t" \
    "ds_write_b64 %[xw], v[96:97] offset:1024\n\t" \
    "s_branch 39f\n\t" \
    "32:\n\t" \
    "13:\n\t" \
    "buffer_load_dwordx4 v[64:67], %[mvo], %[rs], %[so] offen offset:0 sc1\n\t" \
    "buffer_load_dwordx4 v[68:71], %[mvo], %[rs], %[so] offen offset:1024 sc1\n\t" \
    "s_waitcnt vmcnt(1)\n\t" \
    "v_max3_u32 %[t0], v64, v65, v66\n\t" \
    "v_max_u32 %[t0], %[t0], v67\n\t" \
    "s_waitcnt vmcnt(0)\n\t" \
    "v_max3_u32 %[t0], %[t0], v68, v69\n\t" \
    "v_max3_u32 %[t0], %[t0], v70, v71\n\t" \
    "v_cmp_eq_u32 vcc, -1, %[t0]\n\t" \
    "s_cbranch_vccz 23f\n\t" \
    "s_cmp_eq_u32 %[cnt], 0\n\t" \
    "s_cbranch_scc1 23f\n\t" \
    "s_sub_u32 %[cnt], %[cnt], 1\n\t" \
    "s_branch 13b\n\t" \
    "23:\n\t" \
    "s_memrealtime %[tz]\n\t" \
    "v_smfmac_f32_16x16x64_bf16 v[48:51], v[64:67], a[96:103], %[idx]\n\t" \
    "ds_write_b128 %[mw], v[68:71] offset:7168\n\t" \
    "v_smfmac_f32_16x16x64_bf16 v[52:55], v[64:67], a[104:111], %[idx]\n\t" \
    "ds_write_b128 %[mw], v[64:67] offset:6144\n\t" \
    "v_smfmac_f32_16x16x64_bf16 v[56:59], v[64:67], a[224:231], %[idx]\n\t" \
    "v_smfmac_f32_16x16x64_bf16 v[60:63], v[64:67], a[232:239], %[idx]\n\t" \
    "v_smfmac_f32_16x16x64_bf16 v[96:99], v[64:67], v[208:215], %[idx]\n\t" \
    "v_smfmac_f32_16x16x64_bf16 v[100:103], v[64:67], v[216:223], %[idx]\n\t" \
    "s_waitcnt lgkmcnt(0)\n\t" \
    "s_barrier\n\t" \
    "ds_read_b128 v[88:91], %[mw] offset:9216\n\t" \
    "ds_read_b128 v[92:95], %[mw] offset:0\n\t" \
    "v_smfmac_f32_16x16x64_bf16 v[48:51], v[68:71], a[112:119], %[idx]\n\t" \
    "s_mov_b64 vcc, %[tz]\n\t" \
    "v_add_u32 %[t0], vcc_lo, %[dd]\n\t" \
    "ds_read_b128 v[28:31], %[mw] offset:1024\n\t" \
    "v_smfmac_f32_16x16x64_bf16 v[52:55], v[68:71], a[120:127], %[idx]\n\t" \
    "buffer_store_dword %[t0], %[hvo], %[rs], %[sh] offen sc1\n\t" \
    "ds_read_b128 v[32:35], %[mw] offset:2048\n\t" \
    "v_smfmac_f32_16x16x64_bf16 v[56:59], v[68:71], a[240:247], %[idx]\n\t" \
    "ds_read_b128 v[36:39], %[mw] offset:8192\n\t" \
    "v_smfmac_f32_16x16x64_bf16 v[60:63], v[68:71], a[248:255], %[idx]\n\t" \
    "ds_read_b128 v[40:43], %[mw] offset:3072\n\t" \
    "v_smfmac_f32_16x16x64_bf16 v[96:99], v[68:71], v[224:231], %[idx]\n\t" \
    "ds_read_b128 v[44:47], %[mw] offset:4096\n\t" \
    "v_smfmac_f32_16x16x64_bf16 v[100:103], v[68:71], v[232:239], %[idx]\n\t" \
    "ds_read_b128 v[104:107], %[mw] offset:5120\n\t" \
    "s_waitcnt lgkmcnt(7)\n\t" \
    "v_smfmac_f32_16x16x64_bf16 v[48:51], v[88:91], v[72:79], %[idx]\n\t" \
    "ds_read_b128 v[72:75], %[t1] offset:0\n\t" \
    "ds_read_b128 v[76:79], %[t1] offset:1024\n\t" \
    "s_waitcnt lgkmcnt(8)\n\t" \
    "v_smfmac_f32_16x16x64_bf16 v[48:51], v[92:95], a[0:7], %[idx]\n\t" \
    "v_smfmac_f32_16x16x64_bf16 v[52:55], v[92:95], a[8:15], %[idx]\n\t" \
    "s_waitcnt lgkmcnt(7)\n\t" \
    "v_smfmac_f32_16x16x64_bf16 v[48:51], v[28:31], a[16:23], %[idx]\n\t" \
    "v_smfmac_f32_16x16x64_bf16 v[52:55], v[88:91], v[80:87], %[idx]\n\t" \
    "ds_read_b128 v[80:83], %[t1] offset:8192\n\t" \
    "ds_read_b128 v[84:87], %[t1] offset:9216\n\t" \
    "v_smfmac_f32_16x16x64_bf16 v[52:55], v[28:31], a[24:31], %[idx]\n\t" \
    "s_waitcnt lgkmcnt(8)\n\t" \
    "v_smfmac_f32_16x16x64_bf16 v[48:51], v[32:35], a[32:39], %[idx]\n\t" \
    "v_smfmac_f32_16x16x64_bf16 v[52:55], v[32:35], a[40:47], %[idx]\n\t" \
    "s_waitcnt lgkmcnt(2)\n\t" \
    "v_smfmac_f32_16x16x64_bf16 v[48:51], v[36:39], v[72:79], %[idx]\n\t" \
    "ds_read_b128 v[72:75], %[pa] offset:16384\n\t" \
    "ds_read_b128 v[76:79], %[pa] offset:17408\n\t" \
    "v_smfmac_f32_16x16x64_bf16 v[48:51], v[40:43], a[48:55], %[idx]\n\t" \
    "v_smfmac_f32_16x16x64_bf16 v[52:55], v[40:43], a[56:63], %[idx]\n\t" \
    "v_smfmac_f32_16x16x64_bf16 v[48:51], v[44:47], a[64:71], %[idx]\n\t" \
    "s_waitcnt lgkmcnt(2)\n\t" \
    "v_smfmac_f32_16x16x64_bf16 v[52:55], v[36:39], v[80:87], %[idx]\n\t" \
    "ds_read_b128 v[80:83], %[pa] offset:24576\n\t" \
    "ds_read_b128 v[84:87], %[pa] offset:25600\n\t" \
    "v_smfmac_f32_16x16x64_bf16 v[52:55], v[44:47], a[72:79], %[idx]\n\t" \
    "v_smfmac_f32_16x16x64_bf16 v[48:51], v[104:107], a[80:87], %[idx]\n\t" \
    "v_smfmac_f32_16x16x64_bf16 v[52:55], v[104:107], a[88:95], %[idx]\n\t" \
    "s_waitcnt lgkmcnt(2)\n\t" \
    "v_smfmac_f32_16x16x64_bf16 v[56:59], v[88:91], v[72:79], %[idx]\n\t" \
    "ds_read_b128 v[72:75], %[t1] offset:16384\n\t" \
    "ds_read_b128 v[76:79], %[t1] offset:17408\n\t" \
    "v_smfmac_f32_16x16x64_bf16 v[56:59], v[92:95], a[128:135], %[idx]\n\t" \
    "v_smfmac_f32_16x16x64_bf16 v[60:63], v[92:95], a[136:143], %[idx]\n\t" \
    "v_smfmac_f32_16x16x64_bf16 v[56:59], v[28:31], a[144:151], %[idx]\n\t" \
    "s_waitcnt lgkmcnt(2)\n\t" \
    "v_smfmac_f32_16x16x64_bf16 v[60:63], v[88:91], v[80:87], %[idx]\n\t" \
    "ds_read_b128 v[80:83], %[t1] offset:24576\n\t" \
    "ds_read_b128 v[84:87], %[t1] offset:25600\n\t" \
    "v_smfmac_f32_16x16x64_bf16 v[60:63], v[28:31], a[152:159], %[idx]\n\t" \
    "v_smfmac_f32_16x16x64_bf16 v[56:59], v[32:35], a[160:167], %[idx]\n\t" \
    "v_smfmac_f32_16x16x64_bf16 v[60:63], v[32:35], a[168:175], %[idx]\n\t" \
    "v_pk_add_f32 v[48:49], v[48:49], v[50:51]\n\t" \
    "s_waitcnt lgkmcnt(2)\n\t" \
    "v_smfmac_f32_16x16x64_bf16 v[56:59], v[36:39], v[72:79], %[idx]\n\t" \
    "ds_read_b128 v[72:75], %[pa] offset:32768\n\t" \
    "ds_read_b128 v[76:79], %[pa] offset:33792\n\t" \
    "v_pk_add_f32 v[52:53], v[52:53], v[54:55]\n\t" \
    "v_smfmac_f32_16x16x64_bf16 v[56:59], v[40:43], a[176:183], %[idx]\n\t" \
    "v_cvt_pk_bf16_f32 v48, v48, v49\n\t" \
    "v_smfmac_f32_16x16x64_bf16 v[60:63], v[40:43], a[184:191], %[idx]\n\t" \
    "v_cvt_pk_bf16_f32 v49, v52, v53\n\t" \
    "v_smfmac_f32_16x16x64_bf16 v[56:59], v[44:47], a[192:199], %[idx]\n\t" \
    "ds_write_b64 %[xw], v[48:49] offset:0\n\t" \
    "s_waitcnt lgkmcnt(3)\n\t" \
    "v_smfmac_f32_16x16x64_bf16 v[60:63], v[36:39], v[80:87], %[idx]\n\t" \
    "ds_read_b128 v[80:83], %[pa] offset:40960\n\t" \
    "ds_read_b128 v[84:87], %[pa] offset:41984\n\t" \
    "v_smfmac_f32_16x16x64_bf16 v[60:63], v[44:47], a[200:207], %[idx]\n\t" \
    "v_smfmac_f32_16x16x64_bf16 v[56:59], v[104:107], a[208:215], %[idx]\n\t" \
    "v_smfmac_f32_16x16x64_bf16 v[60:63], v[104:107], a[216:223], %[idx]\n\t" \
    "s_waitcnt lgkmcnt(3)\n\t" \
    "v_smfmac_f32_16x16x64_bf16 v[96:99], v[88:91], v[72:79], %[idx]\n\t" \
    "v_smfmac_f32_16x16x64_bf16 v[96:99], v[92:95], v[112:119], %[idx]\n\t" \
    "v_smfmac_f32_16x16x64_bf16 v[100:103], v[92:95], v[120:127], %[idx]\n\t" \
    "v_smfmac_f32_16x16x64_bf16 v[96:99], v[28:31], v[128:135], %[idx]\n\t" \
    "s_waitcnt lgkmcnt(0)\n\t" \
    "v_smfmac_f32_16x16x64_bf16 v[100:103], v[88:91], v[80:87], %[idx]\n\t" \
    "v_smfmac_f32_16x16x64_bf16 v[100:103], v[28:31], v[136:143], %[idx]\n\t" \
    "v_smfmac_f32_16x16x64_bf16 v[96:99], v[32:35], v[144:151], %[idx]\n\t" \
    "v_smfmac_f32_16x16x64_bf16 v[100:103], v[32:35], v[152:159], %[idx]\n\t" \
    "v_pk_add_f32 v[56:57], v[56:57], v[58:59]\n\t" \
    "v_smfmac_f32_16x16x64_bf16 v[96:99], v[36:39], v[240:247], %[idx]\n\t" \
    "v_pk_add_f32 v[60:61], v[60:61], v[62:63]\n\t" \
    "v_smfmac_f32_16x16x64_bf16 v[100:103], v[36:39], v[248:255], %[idx]\n\t" \
    "v_cvt_pk_bf16_f32 v56, v56, v57\n\t" \
    "v_smfmac_f32_16x16x64_bf16 v[96:99], v[40:43], v[160:167], %[idx]\n\t" \
    "v_cvt_pk_bf16_f32 v57, v60, v61\n\t" \
    "v_smfmac_f32_16x16x64_bf16 v[100:103], v[40:43], v[168:175], %[idx]\n\t" \
    "ds_write_b64 %[xw], v[56:57] offset:512\n\t" \
    "v_smfmac_f32_16x16x64_bf16 v[96:99], v[44:47], v[176:183], %[idx]\n\t" \
    "v_smfmac_f32_16x16x64_bf16 v[100:103], v[44:47], v[184:191], %[idx]\n\t" \
    "v_smfmac_f32_16x16x64_bf16 v[96:99], v[104:107], v[192:199], %[idx]\n\t" \
    "v_smfmac_f32_16x16x64_bf16 v[100:103], v[104:107], v[200:207], %[idx]\n\t" \
    "s_nop 7\n\t" \
    "v_pk_add_f32 v[96:97], v[96:97], v[98:99]\n\t" \
    "v_pk_add_f32 v[100:101], v[100:101], v[102:103]\n\t" \
    "v_cvt_pk_bf16_f32 v96, v96, v97\n\t" \
    "v_cvt_pk_bf16_f32 v97, v100, v101\n\t" \
    "ds_write_b64 %[xw], v[96:97] offset:1024\n\t" \
    "39:\n\t" \
    "s_waitcnt lgkmcnt(0)\n\t" \
    "s_barrier\n\t"
#define SCORE_B \
    "s_cmp_lg_u32 %[mode], 0\n\t" \
    "s_cbranch_scc1 3f\n\t" \
    "ds_read_b64_tr_b16 v[96:97], %[xt] offset:0\n\t" \
    "ds_read_b64_tr_b16 v[98:99], %[xt] offset:512\n\t" \
    "ds_read_b64_tr_b16 v[100:101], %[xt] offset:1024\n\t" \
    "ds_read_b64_tr_b16 v[102:103], %[xt] offset:1536\n\t" \
    "ds_read_b64_tr_b16 v[104:105], %[xt] offset:2048\n\t" \
    "ds_read_b64_tr_b16 v[106:107], %[xt] offset:2560\n\t" \
    "ds_read_b64_tr_b16 v[108:109], %[xt] offset:3072\n\t" \
    "ds_read_b64_tr_b16 v[110:111], %[xt] offset:3584\n\t" \
    "ds_read_b64_tr_b16 v[32:33], %[xt] offset:4096\n\t" \
    "ds_read_b64_tr_b16 v[34:35], %[xt] offset:4608\n\t" \
    "ds_read_b64_tr_b16 v[36:37], %[xt] offset:5120\n\t" \
    "ds_read_b64_tr_b16 v[38:39], %[xt] offset:5632\n\t" \
    "s_waitcnt lgkmcnt(10)\n\t" \
    "v_mfma_f32_16x16x32_bf16 v[28:31], v[96:99], v[96:99], 0\n\t" \
    "v_mfma_f32_16x16x32_bf16 v[40:43], v[64:67], v[96:99], 0\n\t" \
    "v_mfma_f32_16x16x32_bf16 v[44:47], v[68:71], v[96:99], 0\n\t" \
    "s_waitcnt lgkmcnt(8)\n\t" \
    "v_mfma_f32_16x16x32_bf16 v[28:31], v[100:103], v[100:103], v[28:31]\n\t" \
    "v_mfma_f32_16x16x32_bf16 v[40:43], v[48:51], v[100:103], v[40:43]\n\t" \
    "v_mfma_f32_16x16x32_bf16 v[44:47], v[52:55], v[100:103], v[44:47]\n\t" \
    "s_waitcnt lgkmcnt(6)\n\t" \
    "v_mfma_f32_16x16x32_bf16 v[28:31], v[104:107], v[104:107], v[28:31]\n\t" \
    "v_mfma_f32_16x16x32_bf16 v[40:43], v[72:75], v[104:107], v[40:43]\n\t" \
    "v_mfma_f32_16x16x32_bf16 v[44:47], v[76:79], v[104:107], v[44:47]\n\t" \
    "s_waitcnt lgkmcnt(4)\n\t" \
    "v_mfma_f32_16x16x32_bf16 v[28:31], v[108:111], v[108:111], v[28:31]\n\t" \
    "v_mfma_f32_16x16x32_bf16 v[40:43], v[80:83], v[108:111], v[40:43]\n\t" \
    "v_mfma_f32_16x16x32_bf16 v[44:47], v[84:87], v[108:111], v[44:47]\n\t" \
    "s_waitcnt lgkmcnt(2)\n\t" \
    "v_mfma_f32_16x16x32_bf16 v[28:31], v[32:35], v[32:35], v[28:31]\n\t" \
    "v_mfma_f32_16x16x32_bf16 v[40:43], v[88:91], v[32:35], v[40:43]\n\t" \
    "v_mfma_f32_16x16x32_bf16 v[44:47], v[92:95], v[32:35], v[44:47]\n\t" \
    "s_waitcnt lgkmcnt(0)\n\t" \
    "v_mfma_f32_16x16x32_bf16 v[28:31], v[36:39], v[36:39], v[28:31]\n\t" \
    "v_mfma_f32_16x16x32_bf16 v[40:43], v[56:59], v[36:39], v[40:43]\n\t" \
    "v_mfma_f32_16x16x32_bf16 v[44:47], v[60:63], v[36:39], v[44:47]\n\t" \
    "s_nop 5\n\t" \
    "v_max_f32 v96, v30, v31\n\t" \
    "v_max3_f32 v97, v40, v41, v42\n\t" \
    "v_max3_f32 v96, v96, v43, v44\n\t" \
    "v_max3_f32 v97, v97, v45, v46\n\t" \
    "v_max3_f32 v96, v96, v97, v47\n\t" \
    "v_mov_b32 v97, v96\n\t" \
    "s_nop 1\n\t" \
    "v_permlane16_swap_b32 v96, v97\n\t" \
    "v_max_f32 v96, v96, v97\n\t" \
    "v_mov_b32 v97, v96\n\t" \
    "s_nop 1\n\t" \
    "v_permlane32_swap_b32 v96, v97\n\t" \
    "v_max_f32 v96, v96, v97\n\t" \
    "v_mul_f32 v96, %[sc], v96\n\t" \
    "v_fma_f32 v40, v40, %[sc], -v96\n\t" \
    "v_fma_f32 v41, v41, %[sc], -v96\n\t" \
    "v_fma_f32 v42, v42, %[sc], -v96\n\t" \
    "v_fma_f32 v43, v43, %[sc], -v96\n\t" \
    "v_fma_f32 v44, v44, %[sc], -v96\n\t" \
    "v_fma_f32 v45, v45, %[sc], -v96\n\t" \
    "v_fma_f32 v46, v46, %[sc], -v96\n\t" \
    "v_fma_f32 v47, v47, %[sc], -v96\n\t" \
    "v_fma_f32 v30, v30, %[sc], -v96\n\t" \
    "v_fma_f32 v31, v31, %[sc], -v96\n\t" \
    "v_exp_f32 v40, v40\n\t" \
    "v_exp_f32 v41, v41\n\t" \
    "v_exp_f32 v42, v42\n\t" \
    "v_exp_f32 v43, v43\n\t" \
    "v_exp_f32 v44, v44\n\t" \
    "v_exp_f32 v45, v45\n\t" \
    "v_exp_f32 v46, v46\n\t" \
    "v_exp_f32 v47, v47\n\t" \
    "v_exp_f32 v30, v30\n\t" \
    "v_exp_f32 v31, v31\n\t" \
    "v_pk_add_f32 v[98:99], v[40:41], v[42:43]\n\t" \
    "v_pk_add_f32 v[100:101], v[44:45], v[46:47]\n\t" \
    "v_pk_add_f32 v[98:99], v[98:99], v[100:101]\n\t" \
    "v_pk_add_f32 v[98:99], v[98:99], v[30:31]\n\t" \
    "v_add_f32 v98, v98, v99\n\t" \
    "v_mov_b32 v99, v98\n\t" \
    "s_nop 1\n\t" \
    "v_permlane16_swap_b32 v98, v99\n\t" \
    "v_add_f32 v98, v98, v99\n\t" \
    "v_mov_b32 v99, v98\n\t" \
    "s_nop 1\n\t" \
    "v_permlane32_swap_b32 v98, v99\n\t" \
    "v_add_f32 v98, v98, v99\n\t" \
    "v_rcp_f32 v98, v98\n\t" \
    "s_xor_b32 %[s1], %[so], 0x2000\n\t" \
    "v_pk_mul_f32 v[40:41], v[40:41], v[98:99] op_sel_hi:[1,0]\n\t" \
    "v_pk_mul_f32 v[42:43], v[42:43], v[98:99] op_sel_hi:[1,0]\n\t" \
    "v_pk_mul_f32 v[44:45], v[44:45], v[98:99] op_sel_hi:[1,0]\n\t" \
    "v_pk_mul_f32 v[46:47], v[46:47], v[98:99] op_sel_hi:[1,0]\n\t" \
    "v_pk_mul_f32 v[30:31], v[30:31], v[98:99] op_sel_hi:[1,0]\n\t" \
    "v_cvt_pk_bf16_f32 v104, |v40|, |v41|\n\t" \
    "v_cvt_pk_bf16_f32 v105, |v42|, |v43|\n\t" \
    "v_cvt_pk_bf16_f32 v106, |v44|, |v45|\n\t" \
    "v_cvt_pk_bf16_f32 v107, |v46|, |v47|\n\t" \
    "v_cvt_pk_bf16_f32 v108, |v30|, |v31|\n\t" \
    "buffer_store_dwordx4 v[104:107], %[pvx], %[rs], %[so] offen sc1\n\t" \
    "buffer_store_dword v108, %[pvm], %[rs], %[so] offen sc1\n\t" \
    "buffer_store_dwordx4 v[104:107], %[pvx], %[rs], %[s1] offen sc1\n\t" \
    "buffer_store_dword v108, %[pvm], %[rs], %[s1] offen sc1\n\t" \
    "s_nop 1\n\t" \
    "s_branch 4f\n\t" \
    "3:\n\t" \
    "s_cmp_lg_u32 %[mode], 1\n\t" \
    "s_cbranch_scc1 4f\n\t" \
    "s_sleep 1\n\t" \
    "ds_read_b128 v[96:99], %[qb] offset:0\n\t" \
    "ds_read_b128 v[100:103], %[qb] offset:1024\n\t" \
    "ds_read_b128 v[104:107], %[qb] offset:2048\n\t" \
    "ds_read_b128 v[108:111], %[qb] offset:3072\n\t" \
    "ds_read_b128 v[32:35], %[qb] offset:4096\n\t" \
    "ds_read_b128 v[36:39], %[qb] offset:5120\n\t" \
    "s_waitcnt lgkmcnt(5)\n\t" \
    "v_mfma_f32_16x16x32_bf16 v[40:43], v[64:67], v[96:99], 0\n\t" \
    "v_mfma_f32_16x16x32_bf16 v[44:47], v[68:71], v[96:99], 0\n\t" \
    "ds_read_b64_tr_b16 v[64:65], %[xt] offset:0\n\t" \
    "ds_read_b64_tr_b16 v[66:67], %[xt] offset:512\n\t" \
    "v_cmp_gt_i32 %[k0], 0, %[dq]\n\t" \
    "v_cmp_gt_i32 %[k4], 16, %[dq]\n\t" \
    "s_waitcnt lgkmcnt(6)\n\t" \
    "v_mfma_f32_16x16x32_bf16 v[40:43], v[48:51], v[100:103], v[40:43]\n\t" \
    "v_mfma_f32_16x16x32_bf16 v[44:47], v[52:55], v[100:103], v[44:47]\n\t" \
    "ds_read_b64_tr_b16 v[48:49], %[xt] offset:1024\n\t" \
    "ds_read_b64_tr_b16 v[50:51], %[xt] offset:1536\n\t" \
    "v_cmp_gt_i32 %[k1], 1, %[dq]\n\t" \
    "v_cmp_gt_i32 %[k5], 17, %[dq]\n\t" \
    "s_waitcnt lgkmcnt(7)\n\t" \
    "v_mfma_f32_16x16x32_bf16 v[40:43], v[72:75], v[104:107], v[40:43]\n\t" \
    "v_mfma_f32_16x16x32_bf16 v[44:47], v[76:79], v[104:107], v[44:47]\n\t" \
    "ds_read_b64_tr_b16 v[72:73], %[xt] offset:2048\n\t" \
    "ds_read_b64_tr_b16 v[74:75], %[xt] offset:2560\n\t" \
    "v_cmp_gt_i32 %[k2], 2, %[dq]\n\t" \
    "v_cmp_gt_i32 %[k6], 18, %[dq]\n\t" \
    "v_mov_b32 v52, 0xd04e79ff\n\t" \
    "s_waitcnt lgkmcnt(8)\n\t" \
    "v_mfma_f32_16x16x32_bf16 v[40:43], v[80:83], v[108:111], v[40:43]\n\t" \
    "v_mfma_f32_16x16x32_bf16 v[44:47], v[84:87], v[108:111], v[44:47]\n\t" \
    "ds_read_b64_tr_b16 v[80:81], %[xt] offset:3072\n\t" \
    "ds_read_b64_tr_b16 v[82:83], %[xt] offset:3584\n\t" \
    "v_cmp_gt_i32 %[k3], 3, %[dq]\n\t" \
    "v_cmp_gt_i32 %[k7], 19, %[dq]\n\t" \
    "s_waitcnt lgkmcnt(9)\n\t" \
    "v_mfma_f32_16x16x32_bf16 v[40:43], v[88:91], v[32:35], v[40:43]\n\t" \
    "v_mfma_f32_16x16x32_bf16 v[44:47], v[92:95], v[32:35], v[44:47]\n\t" \
    "ds_read_b64_tr_b16 v[88:89], %[xt] offset:4096\n\t" \
    "ds_read_b64_tr_b16 v[90:91], %[xt] offset:4608\n\t" \
    "s_waitcnt lgkmcnt(10)\n\t" \
    "v_mfma_f32_16x16x32_bf16 v[40:43], v[56:59], v[36:39], v[40:43]\n\t" \
    "v_mfma_f32_16x16x32_bf16 v[44:47], v[60:63], v[36:39], v[44:47]\n\t" \
    "ds_read_b64_tr_b16 v[56:57], %[xt] offset:5120\n\t" \
    "ds_read_b64_tr_b16 v[58:59], %[xt] offset:5632\n\t" \
    "s_waitcnt lgkmcnt(10)\n\t" \
    "v_mfma_f32_16x16x32_bf16 v[28:31], v[64:67], v[96:99], 0\n\t" \
    "s_waitcnt lgkmcnt(8)\n\t" \
    "v_mfma_f32_16x16x32_bf16 v[28:31], v[48:51], v[100:103], v[28:31]\n\t" \
    "s_waitcnt lgkmcnt(6)\n\t" \
    "v_mfma_f32_16x16x32_bf16 v[28:31], v[72:75], v[104:107], v[28:31]\n\t" \
    "s_waitcnt lgkmcnt(4)\n\t" \
    "v_mfma_f32_16x16x32_bf16 v[28:31], v[80:83], v[108:111], v[28:31]\n\t" \
    "s_waitcnt lgkmcnt(2)\n\t" \
    "v_mfma_f32_16x16x32_bf16 v[28:31], v[88:91], v[32:35], v[28:31]\n\t" \
    "s_waitcnt lgkmcnt(0)\n\t" \
    "v_mfma_f32_16x16x32_bf16 v[28:31], v[56:59], v[36:39], v[28:31]\n\t" \
    "v_cndmask_b32 v40, v40, v52, %[k0]\n\t" \
    "v_cndmask_b32 v41, v41, v52, %[k1]\n\t" \
    "v_cndmask_b32 v42, v42, v52, %[k2]\n\t" \
    "v_cndmask_b32 v43, v43, v52, %[k3]\n\t" \
    "v_cndmask_b32 v44, v44, v52, %[k4]\n\t" \
    "v_cndmask_b32 v45, v45, v52, %[k5]\n\t" \
    "v_cndmask_b32 v46, v46, v52, %[k6]\n\t" \
    "v_cndmask_b32 v47, v47, v52, %[k7]\n\t" \
    "v_max_f32 v96, v30, v31\n\t" \
    "v_max3_f32 v97, v40, v41, v42\n\t" \
    "v_max3_f32 v96, v96, v43, v44\n\t" \
    "v_max3_f32 v97, v97, v45, v46\n\t" \
    "v_max3_f32 v96, v96, v97, v47\n\t" \
    "v_mov_b32 v97, v96\n\t" \
    "s_nop 1\n\t" \
    "v_permlane16_swap_b32 v96, v97\n\t" \
    "v_max_f32 v96, v96, v97\n\t" \
    "v_mov_b32 v97, v96\n\t" \
    "s_nop 1\n\t" \
    "v_permlane32_swap_b32 v96, v97\n\t" \
    "v_max_f32 v96, v96, v97\n\t" \
    "v_mul_f32 v96, %[sc], v96\n\t" \
    "v_fma_f32 v40, v40, %[sc], -v96\n\t" \
    "v_fma_f32 v41, v41, %[sc], -v96\n\t" \
    "v_fma_f32 v42, v42, %[sc], -v96\n\t" \
    "v_fma_f32 v43, v43, %[sc], -v96\n\t" \
    "v_fma_f32 v44, v44, %[sc], -v96\n\t" \
    "v_fma_f32 v45, v45, %[sc], -v96\n\t" \
    "v_fma_f32 v46, v46, %[sc], -v96\n\t" \
    "v_fma_f32 v47, v47, %[sc], -v96\n\t" \
    "v_fma_f32 v30, v30, %[sc], -v96\n\t" \
    "v_fma_f32 v31, v31, %[sc], -v96\n\t" \
    "v_exp_f32 v40, v40\n\t" \
    "v_exp_f32 v41, v41\n\t" \
    "v_exp_f32 v42, v42\n\t" \
    "v_exp_f32 v43, v43\n\t" \
    "v_exp_f32 v44, v44\n\t" \
    "v_exp_f32 v45, v45\n\t" \
    "v_exp_f32 v46, v46\n\t" \
    "v_exp_f32 v47, v47\n\t" \
    "v_exp_f32 v30, v30\n\t" \
    "v_exp_f32 v31, v31\n\t" \
    "v_pk_add_f32 v[98:99], v[40:41], v[42:43]\n\t" \
    "v_pk_add_f32 v[100:101], v[44:45], v[46:47]\n\t" \
    "v_pk_add_f32 v[98:99], v[98:99], v[100:101]\n\t" \
    "v_pk_add_f32 v[98:99], v[98:99], v[30:31]\n\t" \
    "v_add_f32 v98, v98, v99\n\t" \
    "v_mov_b32 v99, v98\n\t" \
    "s_nop 1\n\t" \
    "v_permlane16_swap_b32 v98, v99\n\t" \
    "v_add_f32 v98, v98, v99\n\t" \
    "v_mov_b32 v99, v98\n\t" \
    "s_nop 1\n\t" \
    "v_permlane32_swap_b32 v98, v99\n\t" \
    "v_add_f32 v98, v98, v99\n\t" \
    "v_rcp_f32 v98, v98\n\t" \
    "s_nop 0\n\t" \
    "v_pk_mul_f32 v[40:41], v[40:41], v[98:99] op_sel_hi:[1,0]\n\t" \
    "v_pk_mul_f32 v[42:43], v[42:43], v[98:99] op_sel_hi:[1,0]\n\t" \
    "v_pk_mul_f32 v[44:45], v[44:45], v[98:99] op_sel_hi:[1,0]\n\t" \
    "v_pk_mul_f32 v[46:47], v[46:47], v[98:99] op_sel_hi:[1,0]\n\t" \
    "v_pk_mul_f32 v[30:31], v[30:31], v[98:99] op_sel_hi:[1,0]\n\t" \
    "v_cvt_pk_bf16_f32 v104, |v40|, |v41|\n\t" \
    "v_cvt_pk_bf16_f32 v105, |v42|, |v43|\n\t" \
    "v_cvt_pk_bf16_f32 v106, |v44|, |v45|\n\t" \
    "v_cvt_pk_bf16_f32 v107, |v46|, |v47|\n\t" \
    "v_cvt_pk_bf16_f32 v108, |v30|, |v31|\n\t" \
    "buffer_store_dwordx4 v[104:107], %[pvx], %[rs], %[so] offen sc1\n\t" \
    "buffer_store_dword v108, %[pvm], %[rs], %[so] offen sc1\n\t" \
    "s_nop 1\n\t" \
    "4:\n\t"
#define VC_44_76 "v44", "v45", "v46", "v47", "v48", "v49", "v50", "v51", "v52", "v53", "v54", "v55", "v56", "v57", "v58", "v59", "v60", "v61", "v62", "v63", "v64", "v65", "v66", "v67", "v68", "v69", "v70", "v71", "v72", "v73", "v74", "v75"
#define VC_96_192 "v96", "v97", "v98", "v99", "v100", "v101", "v102", "v103", "v104", "v105", "v106", "v107", "v108", "v109", "v110", "v111", "v112", "v113", "v114", "v115", "v116", "v117", "v118", "v119", "v120", "v121", "v122", "v123", "v124", "v125", "v126", "v127", "v128", "v129", "v130", "v131", "v132", "v133", "v134", "v135", "v136", "v137", "v138", "v139", "v140", "v141", "v142", "v143", "v144", "v145", "v146", "v147", "v148", "v149", "v150", "v151", "v152", "v153", "v154", "v155", "v156", "v157", "v158", "v159", "v160", "v161", "v162", "v163", "v164", "v165", "v166", "v167", "v168", "v169", "v170", "v171", "v172", "v173", "v174", "v175", "v176", "v177", "v178", "v179", "v180", "v181", "v182", "v183", "v184", "v185", "v186", "v187", "v188", "v189", "v190", "v191"
#define VC_28_48 "v28", "v29", "v30", "v31", "v32", "v33", "v34", "v35", "v36", "v37", "v38", "v39", "v40", "v41", "v42", "v43", "v44", "v45", "v46", "v47"
#define VC_96_112 "v96", "v97", "v98", "v99", "v100", "v101", "v102", "v103", "v104", "v105", "v106", "v107", "v108", "v109", "v110", "v111"
template <int I> DEV v4u sub4(v16u v) { return __builtin_shufflevector(v, v, 4 * I, 4 * I + 1, 4 * I + 2, 4 * I + 3); }
DEV v16u cat8(v8u a, v8u b) { return __builtin_shufflevector(a, b, 0, 1, 2, 3, 4, 5, 6, 7, 8, 9, 10, 11, 12, 13, 14, 15); }
DEV void score_role(const Params& P, const int h, const unsigned lb, const int w, const int lane) {
    const int i16 = lane & 15, g = lane >> 4;
    char* ws = P.ws;
    constexpr unsigned SLOT = 53248, L_PA = 0, L_PB = 2 * SLOT, L_X = SLOT, L_K = SLOT + 12288, L_Q0 = SLOT + 36864, L_Q1 = L_PB + 32768, L_M = L_Q1 + 12288;
    const char* pq = P.Wq + h * 384; const char* pkk = P.Wk + h * 384;
    v16u WV[9];
    const unsigned trb = lb + (16 * (g >> 1) + 4 * (g & 1) + (i16 >> 2)) * 800 + (i16 & 3) * 8 + 96 * w;
    auto unit = [&](unsigned slot_off, int t) {
        const unsigned o = trb + slot_off + (t < 3 ? 0 : 384) + 32 * (t % 3);
        v2u r00 = trd(o), r01 = trd(o + 8 * 800), r10 = trd(o + 32 * 800), r11 = trd(o + 40 * 800);
        return v8u{r00.x, r01.x, r00.y, r01.y, r10.x, r11.x, r10.y, r11.y};
    };
    const unsigned pa = lb + L_PA + w * 2048 + lane * 16;
    auto laddr = [&](int l) { return pa + ((l & 2) && l < 8 ? L_PB : 0) + ((l >> 2) * 2 + (l & 1)) * 8192; };
    auto lstore = [&](int l, v8u v) { const unsigned o = laddr(l); lw128(o, lo4(v)); lw128(o + 1024, hi4(v)); };
    sfor<0, 3>([&](auto C) { stage<64, 24, 13>(pq, pkk, 12288, C.v * 64, lb + ((C.v + 1) % 3) * SLOT, w, lane); });
    sfor<0, 8>([&](auto C) {
        constexpr int c = C.v;
        waitvm<26>(); bar();
        sfor<0, 4>([&](auto T) {
            constexpr int p = T.v >> 1, k = T.v & 1, R = (p * 16 + c * 2 + k) * 8;
            const v8u u = unit(((c + 1) % 3) * SLOT, p + 3 * k);
            aw4<R>(u[0], u[1], u[2], u[3]); aw4<R + 4>(u[4], u[5], u[6], u[7]);
        });
        WV[c] = cat8(unit(((c + 1) % 3) * SLOT, 2), unit(((c + 1) % 3) * SLOT, 5));
        bar();
        if constexpr (c + 3 < 10) stage<64, 24, 13>(pq, pkk, 12288, (c + 3) * 64, lb + ((c + 4) % 3) * SLOT, w, lane);
    });
    unsigned dead = SPIN_LIMIT;
    const rsrc_t rh = mkr(ws + OFF_HAND, 64 * 65536), rp = mkr(ws + (w ? OFF_PX : OFF_HAND), w ? 64 * 32 * 2560 : 64 * 65536), rf = mkr(ws + OFF_FLAG, 64 * 192 * 4);
    const int l6 = lane % 6;
    const unsigned fvo = ((l6 < 3 ? 0 : 93) + 3 * h + l6) * 4;
    const char* qkx = ws + OFF_QKX + (size_t)h * QH;
    const unsigned l16 = lane * 16;
    const unsigned nb = near_bits(ws, lane, dead);
    const bool early = __all(ld32(rf, fvo + 768, 0) != 0u);
    waitvm<0>();
    {
        asm volatile("" : "+{v[112:127]}"(WV[0]), "+{v[128:143]}"(WV[1]), "+{v[144:159]}"(WV[2]), "+{v[160:175]}"(WV[3]), "+{v[176:191]}"(WV[4]), "+{v[192:207]}"(WV[5]), "+{v[208:223]}"(WV[6]), "+{v[224:239]}"(WV[7]));
        const rsrc_t rq = mkr(qkx + QS, 24576);
        v4u t[6];
        auto ldt = [&]() {
#pragma unroll
            for (int i = 0; i < 6; ++i) t[i] = ld128(rq, l16, w * 6144 + i * 1024);
        };
        ldt();
        bar();
        lstore(2, unit(0, 0)); lstore(3, unit(0, 3)); lstore(6, unit(0, 1)); lstore(7, unit(0, 4));
        WV[8] = cat8(unit(0, 2), unit(0, 5));
        bar();
        lstore(0, unit(SLOT, 0)); lstore(1, unit(SLOT, 3)); lstore(4, unit(SLOT, 1)); lstore(5, unit(SLOT, 4)); lstore(8, unit(SLOT, 2)); lstore(9, unit(SLOT, 5));
        if (!early) { wait_flags(rf, fvo + 768, true, dead); ldt(); }
        bar();
#pragma unroll
        for (int i = 0; i < 6; ++i) lw128(lb + (w < 2 ? L_Q1 : L_K) + w * 6144 + i * 1024 + l16, t[i]);
        __builtin_amdgcn_s_waitcnt(0x0F70);
        bar();
    }
    const int idx = (lane & 2) ? 0xEEEE : 0x4444, r8 = 2 * (i16 >> 2) + (i16 & 1);
    const unsigned pb = (h >> 3) * 16384 + 3072 + (h & 7) * 640;
    const unsigned pvx = w ? l16 : (i16 & 2) ? 0x80000000u : pb + (g * 8 + r8) * 16;
    const unsigned pvm = (w ? 1024 + (g >> 1) * 128 + i16 * 8 : (i16 & 2) ? 0x80000000u : pb + 512 + ((g >> 1) * 8 + r8) * 8) + (g & 1) * 4;
    const unsigned pxo = (h * 2 + w - 1) * 1280;
    const unsigned mw = lb + L_M + l16;
    const int dq = 16 * (w - 1) + i16 - 4 * g;
    const unsigned xtr = lb + L_X + (4 * g + (i16 >> 2)) * 32 + (i16 & 3) * 8;
    const unsigned xwr = lb + L_X + (48 * w + i16) * 32 + g * 8;
    constexpr float SC = 0.07216878364870322f, L2E = 1.4426950408889634f;
    const unsigned hvo = lane > 1 ? 0x80000000u : 34816 + lane * 8192 + (h >> 3) * 256 + (h & 7) * 16 + w * 4;
    unsigned dd = 140;
    for (int s = 1; s < 64; ++s) {
        const unsigned xo = (s & 1) * 6144;
        const unsigned kb = lb + L_K + (s & 1) * 12288 + l16;
        const unsigned so = s * 65536, sn = so + ((nb >> (s >> 5)) & 1) * 8192;
        v16u r1, r2, r3;
        unsigned t0_, t1_, s1_;
        unsigned long long tz;
        dead = __builtin_amdgcn_readfirstlane(dead);
        unsigned mode = w == 0 ? (s < 63 ? 0 : 2) : (w + 1) >> 1, bso = w ? s * 81920 + pxo : sn, xt = xtr + xo, qb = lb + ((s & 1) ? L_Q1 : L_Q0) + (w - 1) * 6144 + l16;
        asm volatile("" : "+s"(mode), "+s"(bso), "+v"(xt), "+v"(qb));
        asm volatile(SCORE_A
                     : "={v[48:63]}"(r1), "={v[64:79]}"(r2), "={v[80:95]}"(r3), [cnt] "+s"(dead), [t0] "=&v"(t0_), [t1] "=&v"(t1_), [tz] "=&s"(tz)
                     : [rs] "s"(rh), [so] "s"(sn + w * 16384), [mvo] "v"(l16), [pa] "v"(pa), [mw] "v"(mw), [w] "s"(w), [idx] "v"(idx), [xw] "v"(xwr + xo), [kb] "v"(kb), [dd] "v"(dd + (unsigned)(P.mask >> 8 & 255) - 20u), [hvo] "v"(hvo), [sh] "s"(so),
                       "{v[112:127]}"(WV[0]), "{v[128:143]}"(WV[1]), "{v[144:159]}"(WV[2]), "{v[160:175]}"(WV[3]), "{v[176:191]}"(WV[4]), "{v[192:207]}"(WV[5]), "{v[208:223]}"(WV[6]), "{v[224:239]}"(WV[7]), "{v[240:255]}"(WV[8])
                     : "memory", "vcc", "scc", VC_28_48, VC_96_112, ACLOB);
        dead = __builtin_amdgcn_readfirstlane(dead);
        {
            unsigned long long k0, k1, k2, k3, k4, k5, k6, k7;
            asm volatile(SCORE_B
                         : "+{v[48:63]}"(r1), "+{v[64:79]}"(r2), "+{v[80:95]}"(r3), [s1] "=&s"(s1_), [k0] "=&s"(k0), [k1] "=&s"(k1), [k2] "=&s"(k2), [k3] "=&s"(k3), [k4] "=&s"(k4), [k5] "=&s"(k5), [k6] "=&s"(k6), [k7] "=&s"(k7)
                         : [mode] "s"(mode), [xt] "v"(xt), [qb] "v"(qb), [dq] "v"(dq), [rs] "s"(rp), [so] "s"(bso),
                           [pvx] "v"(pvx), [pvm] "v"(pvm), [sc] "s"(SC * L2E)
                         : "memory", "scc", VC_28_48, VC_96_112, ACLOB
                         );
        }
        if (w == 3 && s + 2 < 64) wait_flags(rf, fvo + (s + 2) * 768, true, dead);
        bar();
        {
            const unsigned tb = (unsigned)__builtin_amdgcn_s_memrealtime(), d = tb - (unsigned)tz;
            dd = d < dd ? d : dd;
        }
        v2u hv;
        if (w == 3) asm volatile("buffer_load_dwordx2 %0, %1, %2, %3 offen sc1" : "=&v"(hv) : "v"(34816 + g * 256 + i16 * 8), "s"(rh), "s"(sn) : "memory");
        {
            const int part = w >> 1;
            gldsn<6, true>(qkx + (size_t)(s + 1 + part) * QS + part * 12288 + (w & 1) * 6144, l16, lb + (part ? L_K + (s & 1) * 12288 : (s & 1) ? L_Q0 : L_Q1) + (w & 1) * 6144);
            if (s == 1 && w < 2) gldsn<6, true>(qkx + 2 * QS + 12288 + w * 6144, l16, lb + L_K + w * 6144);
        }
        if (w == 3) {
            asm volatile("s_waitcnt vmcnt(6)" : "+v"(hv)::"memory");
            const unsigned tgt = umax(hv.x, hv.y) + (unsigned)(P.mask >> 16 & 255);
            while (__any(tgt - (unsigned)__builtin_amdgcn_s_memrealtime() < 400u)) {}
        }
        bar();
    }
    waitvm<0>();
}

DEV void value_role(const Params& P, const int j, const unsigned lb, const int w, const int lane) {
    const int i16 = lane & 15, g = lane >> 4;
    char* ws = P.ws;
    constexpr unsigned L_XN = 0, L_RED = 122880, L_M = L_RED + 2048, L_DUM = L_M + 10240;
    const char* xnf = ws + OFF_XNF + w * 10240;
    auto dma_xn = [&](int s) { gldsn<10, true>(xnf + (size_t)s * 40960, lane * 16, lb + L_XN + (s & 1) * 40960 + w * 10240); };
    unsigned dead = SPIN_LIMIT;
    wait_ln(ws, lane, dead);
    const unsigned nb = near_bits(ws, lane, dead);
    dma_xn(0); dma_xn(1);
    v16u UV[4];
    const rsrc_t rz = mkr(ws + OFF_Z, 64 * 1638400);
    const unsigned zb = (j >> 1) * ZJ + w * 10240 + (j & 1) * 128, zvo = (i16 >> 3) * 1024 + g * 256 + (i16 & 7) * 16;
    {
        const char* up = ws + OFF_UP + (size_t)(j * 4 + w) * 81920;
        const rsrc_t ru = mkr(up, 81920);
        unsigned rdy = 0;
        v4u q[4][4];
        auto ldu = [&](auto I) {
            constexpr int t = (I.v / 20 + 3) & 3, kh = I.v % 20;
            if constexpr (kh == 0) wait_u(ws, ((8 * w + (lane & 7)) * 5 + (j >> 4)) * 4, 3u << (2 * t), rdy, dead);
            if constexpr (kh < 16) ald4<(t * 16 + kh) * 4>(ru, lane * 16, (t * 20 + kh) * 1024);
            else vld4(q[t][kh & 3], ru, lane * 16, (t * 20 + kh) * 1024);
        };
        sfor<0, V1A>(ldu);
        asm volatile("s_waitcnt vmcnt(%c4)" : "+v"(q[3][0]), "+v"(q[3][1]), "+v"(q[3][2]), "+v"(q[3][3]) : "i"(V1A - 20) : "memory");
        bar();
        v4f acc[4];
        v4u f[12];
        auto rd = [&](int i) { f[i % 12] = lr128(lb + L_XN + ((i & 3) * 20 + (i >> 2)) * 1024 + lane * 16); };
#pragma unroll
        for (int i = 0; i < 12; ++i) rd(i);
        sfor<0, 80>([&](auto I) {
            constexpr int kh = I.v >> 2, a = I.v & 3;
            SB();
            if constexpr (kh < 16) mf_va<kh == 0, (48 + kh) * 4>(acc[a], f[I.v % 12]); else mf_vv<false>(acc[a], f[I.v % 12], q[3][kh & 3]);
            SB();
            if constexpr (I.v + 12 < 80) rd(I.v + 12);
            if constexpr (I.v % V1B == 0 && V1A + I.v / V1B < 80) ldu(IC<V1A + I.v / V1B>{});
        });
        SB();
        done4(acc[0], acc[1], acc[2], acc[3]);
#pragma unroll
        for (int z = 0; z < 2; ++z) st128(rz, zvo, z * ZSS + zb + 6144, v4u{pk(acc[2 * z][0], acc[2 * z][1]), pk(acc[2 * z][2], acc[2 * z][3]), pk(acc[2 * z + 1][0], acc[2 * z + 1][1]), pk(acc[2 * z + 1][2], acc[2 * z + 1][3])});
        sfor<V1A + 80 / V1B, 80>(ldu);
        asm volatile("s_waitcnt vmcnt(0)" : "+v"(q[0][0]), "+v"(q[0][1]), "+v"(q[0][2]), "+v"(q[0][3]), "+v"(q[1][0]), "+v"(q[1][1]), "+v"(q[1][2]), "+v"(q[1][3]), "+v"(q[2][0]), "+v"(q[2][1]), "+v"(q[2][2]), "+v"(q[2][3])::"memory");
#pragma unroll
        for (int t = 0; t < 4; ++t) UV[t] = cat8(__builtin_shufflevector(q[t][0], q[t][1], 0, 1, 2, 3, 4, 5, 6, 7), __builtin_shufflevector(q[t][2], q[t][3], 0, 1, 2, 3, 4, 5, 6, 7));
    }
    unsigned cpo = (nb & 1) * 8192 + w * 16384;
    const rsrc_t rh = mkr(ws + OFF_HAND, 64 * 65536), rf = mkr(ws + OFF_FLAGX, 65 * 320);
    const int idx = (lane & 8) ? 0xEEEE : 0x4444;
    const int ap = i16 >> 3, r8 = i16 & 7;
    const unsigned pvx = ap * 640 + (g * 8 + r8) * 16;
    const unsigned pvm = (2 * (g >> 1) + ap) * 640 + 512 + ((g & 1) * 8 + r8) * 8;
    const unsigned redw = lb + (ap == (g >> 1) ? L_RED + ((w * 2 + ap) * 16 + r8 * 2 + (g & 1)) * 16 : L_DUM + lane * 16);
    const unsigned redr = lb + L_RED + (lane >> 5) * 1024 + (lane & 31) * 8;
    const unsigned mso = (lane & 3) ? 0x80000000u : mrow((lane & 31) >> 2) + (lane >> 5) * 8192;
    const unsigned l16 = lane * 16;
    dead = __builtin_amdgcn_readfirstlane(dead);
    unsigned pf = 0;
    if (P.mask >> 25 & 1) {
        const rsrc_t rx = mkr(ws + OFF_XNF, 64 * 40960);
#pragma unroll
        for (int i = 0; i < 10; ++i)
            asm volatile("buffer_load_dword %0, %1, %2, %3 offen sc1" : "+v"(pf) : "v"(((j / 3) & 15) * 163840 + w * 4096 + lane * 64), "s"(rx), "s"(i * 16384) : "memory");
    } else {
        const rsrc_t rx = mkr(ws + OFF_XNF + 81920, 62 * 40960);
#pragma unroll
        for (int i = 0; i < 3; ++i)
            asm volatile("buffer_load_dword %0, %1, %2, %3 offen sc1" : "+v"(pf) : "v"((j / 3) * 98304 + i * 32768 + w * 8192 + lane * 128), "s"(rx), "s"(0) : "memory");
    }
    unsigned s_, so_, sz_, sx_, s1_, s2_, s3_, s4_, km0_, t0_, t1_, va_;
    v16u zc; v4u zm;
    asm volatile(VALUE_LOOP
                 : "=&{v[80:95]}"(zc), "=&{v[76:79]}"(zm), [s] "=&s"(s_), [so] "=&s"(so_), [sz] "=&s"(sz_), [sx] "=&s"(sx_), [s1] "=&s"(s1_), [s2] "=&s"(s2_), [s3] "=&s"(s3_), [s4] "=&s"(s4_), [km0] "=&s"(km0_), [cnt] "+s"(dead), [t0] "=&v"(t0_), [t1] "=&v"(t1_), [va] "=&v"(va_), [cpo] "+s"(cpo), "+v"(pf)
                 : [rs] "s"(rh), [rz] "s"(rz), [rf] "s"(rf), [xnf] "s"(ws + OFF_XNF + w * 5120), [lb] "s"(lb + L_XN), [ldx] "s"(lb + L_XN + w * 5120), [cp1] "s"((nb >> 1) * 8192 + w * 16384), [zb] "s"(zb), [fx] "s"(j * 4),
                   [smo] "s"(mcu(j)), [w] "s"(w), [l16] "v"(l16), [idx] "v"(idx), [pvx] "v"(pvx), [pvm] "v"(pvm), [redw] "v"(redw), [redr] "v"(redr), [mso] "v"(mso),
                   [mvo0] "v"(l16), [mvo1] "v"(w < 2 ? l16 : 0x80000000u), [mw0] "v"(lb + L_M + w * 2048 + l16), [mw1] "v"(lb + (w < 2 ? L_M + (8 + w) * 1024 : L_DUM) + l16), [mr] "v"(lb + L_M + g * 256 + ap * 32 + mrow(r8)),
                   [zvo] "v"(zvo), [hvo] "v"(34816 + w * 256 + (lane & 31) * 4),
                   "{v[192:207]}"(UV[0]), "{v[208:223]}"(UV[1]), "{v[224:239]}"(UV[2]), "{v[240:255]}"(UV[3])
                 : "memory", "vcc", "scc", VC_44_76, VC_96_192, ACLOB
                 );
    const rsrc_t rpx = mkr(ws + OFF_PX + 63 * 81920 + w * 20480, 20480), rat = mkr(ws + OFF_FFN + 63 * 40960, 40960), ri = mkr(P.x + 63 * 40960, 40960);
    unsigned ln = l16;
    asm volatile("" : "+v"(ln));
    const unsigned pm = (ln >> 9) * 5120 + 1024 + (ln & 511) / 2;
    const bool lo = ln < 512;
    const unsigned oo = w < 2 && lo ? w * 20480 + (ln & 255) * 80 + j * 16 + (ln >> 5 & 8) : 0x80000000u;
    v4u px[2][10];
    v2u xv, fv;
    for (;;) {
        fence_c();
        unsigned m = 0;
#pragma unroll
        for (int rt = 0; rt < 2; ++rt) {
#pragma unroll
            for (int hh = 0; hh < 8; ++hh) px[rt][hh] = ld128(rpx, ln, (hh * 2 + rt) * 1280);
#pragma unroll
            for (int a = 0; a < 2; ++a) { v2u m0 = ld64(rpx, pm, (a * 2 + rt) * 1280), m1 = ld64(rpx, pm, (a * 2 + rt) * 1280 + 10240); px[rt][8 + a] = v4u{m0.x, m0.y, m1.x, m1.y}; }
        }
        xv = __builtin_amdgcn_raw_buffer_load_b64(ri, oo, 0, 0); fv = ld64(rat, w < 2 && lo ? ln : 0x80000000u, j * 512 + w * 8);
        fence_c();
#pragma unroll
        for (int q = 0; q < 20; ++q) m = umax(m, mx4(px[q / 10][q % 10]));
        if (__all(m != 0xFFFFFFFFu) || dead == 0) break;
        --dead;
    }
    v4f acc[4];
    sfor<0, 20>([&](auto I) {
        constexpr int z = I.v >> 2, rt = I.v & 1, a = (I.v >> 1) & 1;
        if constexpr (z < 4) mf_vv<z == 0>(acc[rt * 2 + a], sub4<z & 3>(zc), px[rt][2 * z + a]); else mf_vv<false>(acc[rt * 2 + a], zm, px[rt][8 + a]);
    });
    done4(acc[0], acc[1], acc[2], acc[3]);
    const unsigned rb = lb + L_DUM + 1024 + ln;
    lw128f(rb + w * 1024, lo ? acc[0] : acc[1]); lw128f(rb + 4096 + w * 1024, lo ? acc[2] : acc[3]);
    bar();
    if (w < 2 && lo) {
        v4f c = ZERO4;
#pragma unroll
        for (int q = 0; q < 8; ++q) c += lr128f(rb + w * 4096 + q * 512);
        v2u o;
        o.x = pk(rbf(blo(xv.x) + rbf(c[0])) + blo(fv.x), rbf(bhi(xv.x) + rbf(c[1])) + bhi(fv.x));
        o.y = pk(rbf(blo(xv.y) + rbf(c[2])) + blo(fv.y), rbf(bhi(xv.y) + rbf(c[3])) + bhi(fv.y));
        *(v2u*)(P.out + 63 * 40960 + oo) = o;
    }
}

template <int MODE>
DEV void xn_helper(const Params& P, const int hb, const unsigned lb, const int w, const int lane) {
    constexpr bool FFN1 = MODE == 1, ZX = MODE == 2;
    const int i16 = lane & 15, g = lane >> 4;
    char* ws = P.ws;
    constexpr unsigned SLOT = 36864;
    v4u WV[4][4];
    unsigned dead = SPIN_LIMIT;
    const rsrc_t rf = mkr(ws + OFF_FLAG, (unsigned)(FLAG_END - OFF_FLAG));
    if constexpr (ZX) {
        wait_ln(ws, lane, dead);
        const char* up = ws + OFF_UP + (size_t)(hb * 4 + w) * 327680 + 61440;
        const rsrc_t ru = mkr(up, 327680);
        unsigned rdy = 0;
        sfor<0, 64>([&](auto I) {
            constexpr int t = I.v >> 4, kh = I.v & 15;
            if constexpr (kh == 0) wait_u(ws, ((4 * (lane & 6) + 6 + (lane & 1)) * 5 + (hb >> 2)) * 4, 3u << (2 * t), rdy, dead);
            ald4<I.v * 4>(ru, lane * 16, t * 81920 + kh * 1024);
        });
#pragma unroll
        for (int t = 0; t < 4; ++t)
#pragma unroll
            for (int c = 0; c < 4; ++c) WV[t][c] = ld128(ru, lane * 16, t * 81920 + (16 + c) * 1024);
    } else {
        const char *p0, *p1; unsigned ldb;
        if constexpr (FFN1) { const int cb = hb % 12; p0 = P.Wg + cb * 256; p1 = P.Wu + cb * 256; ldb = 3072; }
        else { p0 = (hb < 24 ? P.Wq : P.Wk) + (hb % 24) * 512; p1 = p0 + 256; ldb = 12288; }
        const unsigned trb = lb + (16 * (g >> 1) + 4 * (g & 1) + (i16 >> 2)) * 544 + (i16 & 3) * 8 + (FFN1 ? 64 * w : 128 * w);
        v4u xa[10];
        if constexpr (!FFN1) ln_load(P.x, (hb * 4 + w) * 8, lane, xa);
        else wait_slow(rf, (unsigned)(OFF_FLAGX - OFF_FLAG) + 640 + lane * 4, w, (int)(P.mask >> 40 & 255), dead);
        sfor<0, 4>([&](auto C) { stage<64, 16, 9>(p0, p1, ldb, C.v * 64, lb + C.v * SLOT, w, lane); });
        if constexpr (!FFN1) ln_finish<36>(ws, (hb * 4 + w) * 8, lane, xa);
        sfor<0, 10>([&](auto C) {
            constexpr int c = C.v;
            waitvm<(c <= 6 ? (!FFN1 && c < 4 ? 37 : 27) : (9 - c) * 9)>(); bar();
            if constexpr (!FFN1 && c == 4) if (w == 0 && lane == 0) st32(rf, (unsigned)(OFF_INIT - OFF_FLAG) + (160 + hb) * 4, 0, 1u);
            sfor<0, 8>([&](auto I) {
                constexpr int t = I.v >> 1, k = I.v & 1, kh = 2 * c + k;
                const unsigned o = trb + (c % 4) * SLOT + (FFN1 ? (t >> 1) * 256 + (t & 1) * 32 : t * 32) + k * 32 * 544;
                v2u r0 = trd(o), r1 = trd(o + 8 * 544);
                if constexpr (kh < 16) aw4<(t * 16 + kh) * 4>(r0.x, r1.x, r0.y, r1.y); else WV[t][kh - 16] = v4u{r0.x, r1.x, r0.y, r1.y};
            });
            bar();
            if constexpr (c + 4 < 10) stage<64, 16, 9>(p0, p1, ldb, (c + 4) * 64, lb + (c % 4) * SLOT, w, lane);
        });
        wait_ln(ws, lane, dead);
    }
    const int s0 = FFN1 ? hb / 12 : ZX ? 2 : -1, sstep = FFN1 ? 2 : 1;
    const char* xnf = ws + OFF_XNF + w * 10240;
    auto dma_xn = [&](int s, int buf) { gldsn<10, true>(xnf + s * 40960, lane * 16, lb + buf * 40960 + w * 10240); };
    const rsrc_t ro = FFN1 ? mkr(ws + OFF_HF, 128 * 48 * 1024) : ZX ? mkr(ws + OFF_Z, 64 * 1638400) : mkr(ws + OFF_QKX - QS, 65 * QS);
    unsigned ovo, fo, fstep;
    if constexpr (FFN1) { const int kf = (hb % 12) * 4 + w; ovo = kf * 1024 + lane * 16; fo = 64 * 192 * 4 + kf * 4; fstep = 256; }
    else if constexpr (ZX) { const int j = hb * 4 + w; ovo = (j >> 1) * ZJ + (j & 1) * 128 + 6144 + (i16 >> 3) * 1024 + g * 256 + (i16 & 7) * 16; fo = (unsigned)(OFF_FLAGX - OFF_FLAG) + j * 4; fstep = 320; }
    else { const int col = (hb % 24) * 256 + 64 * w, hd = col / 192, n0 = (col - hd * 192) >> 5; ovo = hd * QH + (hb < 24 ? 0 : 12288) + n0 * 1024 + lane * 16; fo = (hb * 4 + w) * 4; fstep = 768; }
    const rsrc_t rp = mkr(ws, (unsigned)OFF_FFN);
    v4u ones = {~0u, ~0u, ~0u, ~0u};
    asm volatile("" : "+v"(ones));
    unsigned pvo = 0x80000000u;
    if constexpr (ZX) { if (lane < 16) pvo = 65536 + mcu(hb * 4 + w) + (lane >> 3) * 8192 + mrow(lane & 7); }
    else if constexpr (!FFN1) {
        const int col = (hb % 24) * 256 + 64 * w, hd = col / 192;
        if (col == hd * 192) {
            if (lane <= 40) pvo = (hb < 24 ? 0 : 8192) + (lane < 40 ? (hd >> 3) * 16384 + 3072 + (hd & 7) * 640 + lane * 16 : 34816 + (hd >> 3) * 256 + (hd & 7) * 16);
#pragma unroll
            for (int i = 0; i < 3; ++i) if (i * 64 + lane < 160) st128(rp, (i * 64 + lane) * 16, (unsigned)OFF_PX + (hb < 24 ? 62 : 63) * 81920 + hd * 2560, ones);
        }
    }
    dma_xn(s0, 0);
    waitvm<0>();
    int buf = 0;
    for (int s = s0; s < 64; s += sstep, buf ^= 1) {
        bar();
        const unsigned bb = lb + buf * 40960 + lane * 16;
        v4u f[4];
#pragma unroll
        for (int i = 0; i < 4; ++i) f[i] = lr128(bb + i * 1024);
        if (s + sstep < 64) dma_xn(s + sstep, buf ^ 1);
        v4f acc[2][4];
        auto epi = [&](auto TT) {
            constexpr int tt = TT.v;
            if constexpr (FFN1) {
                float hv[8];
#pragma unroll
                for (int t = 0; t < 2; ++t)
#pragma unroll
                    for (int i = 0; i < 4; ++i) { float gt = rbf(acc[tt][t][i]), up = rbf(acc[tt][2 + t][i]); gt = gt * (gt > 0.f ? 1.f : 0.f); hv[4 * t + i] = gt * up; }
                st128(ro, ovo, (2 * s + tt) * 49152, v4u{pk(hv[0], hv[1]), pk(hv[2], hv[3]), pk(hv[4], hv[5]), pk(hv[6], hv[7])});
            } else if constexpr (ZX) {
                if constexpr (tt == 1) {
#pragma unroll
                    for (int t = 0; t < 4; ++t)
                        st128(ro, ovo + t * 10240, s * ZSS, v4u{pk(acc[0][t][0], acc[0][t][1]), pk(acc[0][t][2], acc[0][t][3]), pk(acc[1][t][0], acc[1][t][1]), pk(acc[1][t][2], acc[1][t][3])});
                }
            } else {
#pragma unroll
                for (int q = 0; q < 2; ++q)
                    st128(ro, ovo + tt * 6144 + q * 1024, (s + 1) * QS, v4u{pk(acc[tt][2 * q][0], acc[tt][2 * q][1]), pk(acc[tt][2 * q][2], acc[tt][2 * q][3]), pk(acc[tt][2 * q + 1][0], acc[tt][2 * q + 1][1]), pk(acc[tt][2 * q + 1][2], acc[tt][2 * q + 1][3])});
            }
        };
        auto grp = [&](auto K) {
            constexpr int k = K.v, tt = k / 20, kh = k % 20;
            SB();
            sfor<0, 4>([&](auto T) {
                constexpr int t = T.v;
                if constexpr (ZX) { if constexpr (kh < 16) mf_va<kh == 0, (t * 16 + kh) * 4>(acc[tt][t], f[k & 3]); else mf_vv<false>(acc[tt][t], f[k & 3], WV[t][kh - 16]); }
                else { if constexpr (kh < 16) mf_av<kh == 0, (t * 16 + kh) * 4>(acc[tt][t], f[k & 3]); else mf_vv<false>(acc[tt][t], WV[t][kh - 16], f[k & 3]); }
            });
            SB();
            if constexpr (k + 4 < 40) f[k & 3] = lr128(bb + (k + 4) * 1024);
            if constexpr (k == 22) {
                waitvm<0>();
                if (s > (MODE ? s0 : 0) && lane == 0) st32(rf, fo, (s - sstep) * fstep, 1u);
                epi(IC<0>{});
            }
        };
        sfor<0, 23>(grp);
        if (MODE == 0 && s < 0) continue;
        sfor<23, 40>(grp);
        SB();
        done4(acc[1][0], acc[1][1], acc[1][2], acc[1][3]);
        epi(IC<1>{});
        if constexpr (!FFN1) st128(rp, pvo, ZX && s == 63 ? 0x80000000u : s * 65536, ones);
        if constexpr (ZX) if (s == 8) {
#pragma unroll 1
            for (int i = 0; i < 20; ++i) st128(ro, lane * 16, 62 * ZSS + (hb * 2 + (w >> 1)) * ZJ + (w & 1) * 20480 + i * 1024, ones);
        }
        if (s < (ZX ? 3 : 2)) { waitvm<0>(); if (lane == 0) st32(rf, fo, s * fstep, 1u); }
    }
    waitvm<0>();
    if (lane == 0) st32(rf, fo, (FFN1 ? 62 + s0 : 63) * fstep, 1u);
}

DEV void ffn2_role(const Params& P, const int fb, const unsigned lb, const int w, const int lane) {
    const int i16 = lane & 15, g = lane >> 4;
    char* ws = P.ws;
    constexpr unsigned SLOT = 36864;
    const int grp = fb / 5, db = fb % 5;
    const char* p0 = P.Wd + db * 256; const char* p1 = p0 + 128;
    v4u WV[2][16];
    const unsigned trb = lb + (4 * g + (i16 >> 2)) * 288 + (i16 & 3) * 8 + 64 * w;
    unsigned dead = SPIN_LIMIT;
    wait_slow(mkr(ws + OFF_FLAGX, 64 * 320), 1280 + lane * 4, w, (int)(P.mask >> 40 & 255), dead);
    sfor<0, 4>([&](auto C) { stage<128, 8, 9>(p0, p1, 1280, C.v * 128, lb + C.v * SLOT, w, lane); });
    sfor<0, 12>([&](auto C) {
        constexpr int c = C.v;
        waitvm<(c <= 8 ? 27 : (11 - c) * 9)>(); bar();
        sfor<0, 8>([&](auto I) {
            constexpr int t = I.v >> 2, k = I.v & 3, kf = 4 * c + k;
            const unsigned o = trb + (c % 4) * SLOT + t * 32 + k * 32 * 288;
            v2u r0 = trd(o), r1 = trd(o + 16 * 288);
            if constexpr (kf < 32) aw4<(t * 32 + kf) * 4>(r0.x, r0.y, r1.x, r1.y); else WV[t][kf - 32] = v4u{r0.x, r0.y, r1.x, r1.y};
        });
        bar();
        if constexpr (c + 4 < 12) stage<128, 8, 9>(p0, p1, 1280, (c + 4) * 128, lb + (c % 4) * SLOT, w, lane);
    });
    const rsrc_t rf = mkr(ws + OFF_FLAGH, (unsigned)(OFF_INIT - OFF_FLAGH)), rat = mkr(ws + OFF_FFN, 2048 * 1280);
    const unsigned fo = (unsigned)(OFF_FLAGF - OFF_FLAGH) + (db * 4 + w) * 4;
    const char* hf = ws + OFF_HF + w * 24576;
    const unsigned oo = (db * 8 + w * 2) * 1024 + lane * 16;
    wait_flags(rf, grp * 256 + lane * 4, lane < 48, dead);
    gldsn<24, true>(hf + (size_t)grp * 98304, lane * 16, lb + w * 24576);
    for (int s = grp; s < 64; s += 2) {
        if (s > 1) waitvm<2>(); else waitvm<0>();
        bar();
        const unsigned nf = ld32(rf, lane * 4, (s + 2) * 256);
        const unsigned bb = lb + lane * 16;
        v4f acc[2][2];
        v4u f[8];
#pragma unroll
        for (int i = 0; i < 8; ++i) f[i] = lr128(bb + i * 1024);
        sfor<0, 96>([&](auto K) {
            constexpr int k = K.v, tt = k / 48, kf = k % 48;
            SB();
            sfor<0, 2>([&](auto T) {
                constexpr int t = T.v;
                if constexpr (kf < 32) mf_av<kf == 0, (t * 32 + kf) * 4>(acc[tt][t], f[k & 7]); else mf_vv<false>(acc[tt][t], WV[t][kf - 32], f[k & 7]);
            });
            SB();
            if constexpr (k + 8 < 96) f[k & 7] = lr128(bb + (k + 8) * 1024);
        });
        SB();
        done4(acc[0][0], acc[0][1], acc[1][0], acc[1][1]);
        bar();
        waitvm<0>();
        if (s > 1 && lane == 0) st32(rf, fo, (s - 2) * 128, 1u);
        if (s + 2 < 64) {
            if (!__all(lane >= 48 || nf != 0u)) wait_flags(rf, (s + 2) * 256 + lane * 4, lane < 48, dead);
            gldsn<24, true>(hf + (size_t)(s + 2) * 98304, lane * 16, lb + w * 24576);
        }
#pragma unroll
        for (int t = 0; t < 2; ++t) {
            const v4f c = acc[0][t], d = acc[1][t];
            st128(rat, oo + t * 1024, s * 40960, v4u{pk(c[0], c[1]), pk(c[2], c[3]), pk(d[0], d[1]), pk(d[2], d[3])});
        }
    }
    waitvm<0>();
    if (lane < 4) st32(rf, grp ? (unsigned)(OFF_FLAGX - OFF_FLAGH) + 64 * 320 + (db * 16 + w * 4 + lane) * 4 : fo + 62 * 128, 0, 1u);
}

DEV void comb_role(const Params& P, const int J, const int s0, const int s1, const unsigned lb, const int w, const int lane) {
    const int i16 = lane & 15, g = lane >> 4;
    char* ws = P.ws;
    unsigned dead = SPIN_LIMIT;
    const rsrc_t rpx = mkr(ws + OFF_PX, 64 * 81920), rz = mkr(ws + OFF_Z, 64 * 1638400), rat = mkr(ws + OFF_FFN, 2048 * 1280), rh = mkr(ws, (unsigned)FLAG_END), rx = mkr(P.x, 2048 * 1280);
    const bool fl = lane & 2;
    const unsigned nb = near_bits(ws, lane, dead), ffo = (unsigned)OFF_FLAGF + (J >> 1) * 4, mvo = 131072 + mcu(2 * J + (lane & 1)), bad = fl ? 0u : ~0u;
    const unsigned l16 = lane * 16;
    const unsigned pm = (g >> 1) * 5120 + 1024 + (g & 1) * 128 + i16 * 8;
    const unsigned oo = w < 2 ? (w * 16 + i16) * 1280 + J * 32 + g * 8 : 0x80000000u;
    auto cond = [&](int s) { fence_c(); return ld32(rh, fl ? ffo + s * 128 : mvo + s * 65536 + ((nb >> ((s + 2) >> 5)) & 1) * 8192, 0); };
    auto spin = [&](int s) { while (!__all(cond(s) != bad) && dead) { --dead; __builtin_amdgcn_s_sleep(4); } };
    v4u z[10], px[2][10];
    auto ldp = [&](int s) {
        const unsigned so = s * 81920 + w * 20480;
#pragma unroll
        for (int rt = 0; rt < 2; ++rt) {
#pragma unroll
            for (int hh = 0; hh < 8; ++hh) px[rt][hh] = ld128(rpx, l16, so + (hh * 2 + rt) * 1280);
#pragma unroll
            for (int a = 0; a < 2; ++a) { v2u m0 = ld64(rpx, pm, so + (a * 2 + rt) * 1280), m1 = ld64(rpx, pm, so + (a * 2 + rt) * 1280 + 10240); px[rt][8 + a] = v4u{m0.x, m0.y, m1.x, m1.y}; }
        }
    };
    v2u xn, fn;
    auto ldz = [&](int s) {
#pragma unroll
        for (int hh = 0; hh < 10; ++hh) z[hh] = ld128(rz, l16, s * ZSS + J * ZJ + w * 10240 + hh * 1024);
    };
    auto issue = [&](int s) {
        fence_c();
        xn = __builtin_amdgcn_raw_buffer_load_b64(rx, oo, s * 40960, 0); fn = ld64(rat, w < 2 ? l16 : 0x80000000u, s * 40960 + J * 1024 + w * 8);
        ldp(s); ldz(s);
        fence_c();
    };
    wait_slow(rh, fl ? ffo + s0 * 128 : (unsigned)OFF_FLAGX + (s0 ? 62 : 1) * 320 + (2 * J + (lane & 1)) * 4, w, (int)(P.mask >> 40 & 255), dead);
    if (P.mask >> 24 & 1) { if (w == 0) spin(s0 < 61 ? s0 : 61); bar(); } else
    spin(s0 < 61 ? s0 : 61);
    issue(s0);
    for (int s = s0; s < s1; ++s) {
        const unsigned cv = cond(s + 1);
        if (s == 62) {
            for (;;) {
                unsigned m = 0;
#pragma unroll
                for (int q = 0; q < 20; ++q) m = umax(m, mx4(px[q / 10][q % 10]));
                if (__all(m != 0xFFFFFFFFu) || dead == 0) break;
                --dead;
                fence_c(); ldp(s); fence_c();
            }
            for (;;) {
                unsigned m = 0;
#pragma unroll
                for (int hh = 0; hh < 10; ++hh) if (hh != 6 && hh != 7) m = umax(m, mx4(z[hh]));
                if (__all(m != 0xFFFFFFFFu) || dead == 0) break;
                --dead;
                fence_c(); ldz(s); fence_c();
            }
        }
        v4f acc[2];
        sfor<0, 20>([&](auto I) { constexpr int hh = I.v % 10, rt = I.v / 10; mf_vv<hh == 0>(acc[rt], z[hh], px[rt][hh]); });
        v2u xv = xn, fv = fn;
        asm volatile("" : "+v"(xv), "+v"(fv));
        const bool ok = __all(cv != bad);
        if (ok && s + 1 < s1) issue(s + 1);
        done2(acc[0], acc[1]);
        const unsigned rb = lb + (s & 1) * 8192 + l16;
        lw128f(rb + w * 1024, acc[0]); lw128f(rb + 4096 + w * 1024, acc[1]);
        bar();
        if (w < 2) {
            const v4f r0 = lr128f(rb + w * 4096), r1 = lr128f(rb + w * 4096 + 1024), r2 = lr128f(rb + w * 4096 + 2048), r3 = lr128f(rb + w * 4096 + 3072);
            const v4f c = (r0 + r1) + (r2 + r3);
            v2u o;
            o.x = pk(rbf(blo(xv.x) + rbf(c[0])) + blo(fv.x), rbf(bhi(xv.x) + rbf(c[1])) + bhi(fv.x));
            o.y = pk(rbf(blo(xv.y) + rbf(c[2])) + blo(fv.y), rbf(bhi(xv.y) + rbf(c[3])) + bhi(fv.y));
            *(v2u*)(P.out + (size_t)s * 40960 + oo) = o;
        }
        if (!ok && s + 1 < s1) { spin(s + 1); issue(s + 1); }
    }
}

DEV void p0_task(const Params& P, const int h, const int w, const int lane) {
    const int i16 = lane & 15, g = lane >> 4, l6 = lane % 6;
    char* ws = P.ws;
    unsigned dead = SPIN_LIMIT;
    if (w == 3) return;
    wait_flags(mkr(ws + OFF_FLAG, 768), ((l6 < 3 ? 0 : 93) + 3 * h + l6) * 4, true, dead);
    const rsrc_t rq = mkr(ws + OFF_QKX - QS + (size_t)h * QH, 2 * QS);
    v4u q[6], k[3][6];
#pragma unroll
    for (int n = 0; n < 6; ++n) {
        q[n] = ld128(rq, lane * 16, (w == 0 ? 0 : QS + (w - 1) * 6144) + n * 1024);
        k[0][n] = ld128(rq, lane * 16, QS + 12288 + n * 1024); k[1][n] = ld128(rq, lane * 16, QS + 18432 + n * 1024); k[2][n] = ld128(rq, lane * 16, 12288 + n * 1024);
    }
    v4f t[3];
    sfor<0, 18>([&](auto I) { constexpr int n = I.v / 3, a = I.v % 3; mf_vv<n == 0>(t[a], k[a][n], q[n]); });
    done3(t[0], t[1], t[2]);
    constexpr float SC = 0.07216878364870322f * 1.4426950408889634f;
    const int dq = w ? 16 * (w - 1) + i16 - 4 * g : 64;
    float m = -INFINITY, sum = 0.f;
#pragma unroll
    for (int i = 0; i < 12; ++i) {
        float& v = ((float*)t)[i];
        if (i < 8) v = (i & 3) + (i >> 2) * 16 > dq ? __uint_as_float(0xd04e79ffu) : v; else v = g < 2 ? v : -INFINITY;
        m = fmaxf(m, v);
    }
    m = xmax(m) * SC;
#pragma unroll
    for (int i = 0; i < 12; ++i) { float& v = ((float*)t)[i]; v = __builtin_amdgcn_exp2f(fmaf(v, SC, -m)); sum += v; }
    const float r = __builtin_amdgcn_rcpf(xsum(sum));
    unsigned o[6];
#pragma unroll
    for (int i = 0; i < 6; ++i) o[i] = pk(fabsf(((float*)t)[2 * i] * r), fabsf(((float*)t)[2 * i + 1] * r));
    const v4u ox = {o[0], o[1], o[2], o[3]}; const v2u om = {o[4], o[5]};
    const rsrc_t rh = mkr(ws + OFF_HAND, 65536);
    if (w) {
        const rsrc_t rp = mkr(ws + OFF_PX + (h * 2 + w - 1) * 1280, 1280);
        st128(rp, lane * 16, 0, ox); st64(rp, g < 2 ? 1024 + g * 128 + i16 * 8 : 0x80000000u, 0, om);
        waitvm<0>();
    } else {
        const unsigned pb = (h >> 3) * 16384 + 3072 + (h & 7) * 640;
#pragma unroll
        for (int c = 0; c < 2; ++c) {
            st128(rh, i16 < 8 ? pb + (g * 8 + i16) * 16 : 0x80000000u, c * 8192, ox);
            st64(rh, i16 < 8 && g < 2 ? pb + 512 + (g * 8 + i16) * 8 : 0x80000000u, c * 8192, om);
        }
    }
    st32(rh, lane < (w ? 2 : 4) ? 34816 + (lane & 1) * 8192 + (h >> 3) * 256 + (h & 7) * 16 + (w + (lane >> 1) * 3) * 4 : 0x80000000u, 0, canon((unsigned)__builtin_amdgcn_s_memrealtime()));
}

DEV int lane_id() { unsigned l; asm volatile("v_mbcnt_lo_u32_b32 %0, -1, 0\n\tv_mbcnt_hi_u32_b32 %0, -1, %0" : "=v"(l)); __builtin_assume(l < 64u); return l; }
__global__ void __launch_bounds__(256, 1) main_kernel(Params P) {
    extern __shared__ __attribute__((aligned(16))) char lds[];
    const int b = vblk(), tid = threadIdx.x, lane = tid & 63, w = __builtin_amdgcn_readfirstlane(tid >> 6);
    const unsigned lb = (unsigned)(size_t)(LDSP(char))lds;
    {
        const int x = blockIdx.x & 7, i = blockIdx.x >> 3;
        if (x >= 4 ? i >= 12 : x && i < (x == 3 ? 30 : 25)) u_task(P, x >= 4 ? x * 20 + i - 12 : (x - 1) * 25 + i, lb, w, lane);
        else if (x && x < 4) ln_task(P, x == 3 ? i - 16 : (x - 1) * 7 + i - 25, w, lane);
    }
    if (b < 32) { if (P.mask & 1) score_role(P, b, lb, w, lane_id()); }
    else if (b < 112) { if (P.mask & 1) value_role(P, b - 32, lb, w, lane_id()); }
    else if (b < 160) { if (P.mask & 2) xn_helper<0>(P, b - 112, lb, w, lane_id()); }
    else if (b < 184) { if (P.mask & 2) xn_helper<1>(P, b - 160, lb, w, lane_id()); }
    else if (b < 194) { if (P.mask & 2) ffn2_role(P, b - 184, lb, w, lane_id()); }
    else if (b < 214) { if (P.mask & 2) xn_helper<2>(P, b - 194, lb, w, lane_id()); }
    else if (b < 246) p0_task(P, b - 214, w, lane_id());
    const int cj = b < 152 ? b - 112 : b < 214 ? -1 : b < 254 ? b - 214 : -1, cs = b < 152 ? 62 : 0;
    if (cj >= 0) comb_role(P, cj, cs, cs ? 63 : 62, lb, w, lane_id());
}

extern "C" void kernel_launch(void* const* d_in, const int* in_sizes, int n_in, void* d_out, int out_size, void* d_ws, size_t ws_size, hipStream_t stream) {
    Params P;
    P.x = (const char*)d_in[0]; P.mem0 = (const char*)d_in[1]; P.Wq = (const char*)d_in[2]; P.Wk = (const char*)d_in[3]; P.Wv = (const char*)d_in[4]; P.Wo = (const char*)d_in[5];
    P.Wg = (const char*)d_in[8]; P.Wu = (const char*)d_in[9]; P.Wd = (const char*)d_in[10]; P.out = (char*)d_out;
    P.ws = (char*)(((uintptr_t)d_ws + ((1u << 21) - 1)) & ~(uintptr_t)((1u << 21) - 1));
    P.mask = 3 | (C1 << 8) | (D2 << 16) | ((unsigned long long)Q3 << 24) | (1ull << 32) | ((unsigned long long)E4 << 40);
    (void)hipFuncSetAttribute((const void*)main_kernel, hipFuncAttributeMaxDynamicSharedMemorySize, LDS_BYTES);
    prep_kernel<<<80, 256, 0, stream>>>(P);
    main_kernel<<<256, 256, LDS_BYTES, stream>>>(P);
}
```
